```python
import torch

def _softmax(x, axis=-1):
    return torch.softmax(x, dim=axis)
def _gelu(x):
    return torch.nn.functional.gelu(x, approximate="tanh")
def _silu(x):
    return torch.nn.functional.silu(x)
def _sigmoid(x):
    return torch.sigmoid(x)
def _layernorm(x, axis=-1, eps=1e-5):
    m = x.mean(dim=axis, keepdim=True)
    v = x.var(dim=axis, keepdim=True, unbiased=False)
    return (x - m) / torch.sqrt(v + eps)
def _scatter_add(tgt, idx, src, axis=0):
    return tgt.clone().index_add_(axis, idx, src)
def _segment_sum(src, seg, n):
    out = torch.zeros((n,) + src.shape[1:], dtype=src.dtype)
    return out.index_add_(0, seg, src)
def _pad(x, pw):
    flat = tuple(v for pair in reversed(pw) for v in pair)
    return torch.nn.functional.pad(x, flat)

kernel_name = 'concat_heads__softmax_full_mha__ssm_conv_h3__prompt'

def setup_inputs():

    _w = (lambda a: (lambda t: t.to(torch.bfloat16) if t.is_floating_point() else t)(torch.as_tensor(a)))
    def _A_setup():
        B, S, D = 1, 2048, 576
        return {
            "x": _w((lambda *s: torch.randn(*s, dtype=torch.bfloat16) * 0.2)(B, S, D)),
            "wqkv": _w((lambda *s: torch.randn(*s, dtype=torch.bfloat16) * 0.2)(D, 3 * D)),
            "wo": _w((lambda *s: torch.randn(*s, dtype=torch.bfloat16) * 0.05)(D, D)),
        }
    _A_in = _A_setup()

    def _B_setup():
        B, S, D = 1, 2048, 576
        ks = 4
        return {
            "q": _w((lambda *s: torch.randn(*s, dtype=torch.bfloat16) * 0.05)(B, S, D)),
            "k": _w((lambda *s: torch.randn(*s, dtype=torch.bfloat16) * 0.4)(B, S, D)),
            "v": _w((lambda *s: torch.randn(*s, dtype=torch.bfloat16) * 0.4)(B, S, D)),
            "shift_k": _w((lambda *s: torch.randn(*s, dtype=torch.bfloat16) * 0.4)(D, ks) * 0.5),
            "ssm_k": _w((lambda *s: torch.randn(*s, dtype=torch.bfloat16) * 1.6)(D, S) * (1.0 / S ** 0.5)),
            "ssm_D": _w((lambda *s: torch.randn(*s, dtype=torch.bfloat16) * 0.4)(D)),
        }
    _B_in = _B_setup()

    return {
        "A_x": _A_in["x"],
        "A_wqkv": _A_in["wqkv"],
        "A_wo": _A_in["wo"],
        "B_q": _B_in["q"],
        "B_k": _B_in["k"],
        "B_v": _B_in["v"],
        "B_shift_k": _B_in["shift_k"],
        "B_ssm_k": _B_in["ssm_k"],
        "B_ssm_D": _B_in["ssm_D"],
    }

def reference(A_x, A_wqkv, A_wo, B_q, B_k, B_v, B_shift_k, B_ssm_k, B_ssm_D):
    def _A_ref(x, wqkv, wo):
        B, S, D = x.shape
        H = 32; Dh = D // H
        qkv = (lambda x, *d: x.permute(*d))((x @ wqkv).reshape(B, S, 3, H, Dh), 2, 0, 3, 1, 4)
        q, k, v = qkv[0], qkv[1], qkv[2]
        scale = Dh ** -0.5
        scores = torch.einsum("bhqd,bhkd->bhqk", q, k) * scale
        p = _softmax(scores, axis=-1)
        o = torch.einsum("bhqk,bhkd->bhqd", p, v)
        return (lambda x, *d: x.permute(*d))(o, 0, 2, 1, 3).reshape(B, S, D) @ wo

    def _B_ref(q, k, v, shift_k, ssm_k, ssm_D):
        B, S, D = q.shape
        ks = shift_k.shape[-1]
        k_pad = _pad(k, ((0, 0), (ks - 1, 0), (0, 0)))
        k_shift = torch.zeros_like(k)
        for t in range(ks):
            k_shift = k_shift + k_pad[:, t:t + S, :] * shift_k[:, ks - 1 - t][None, None, :]
        kv = k_shift * v
        L = 2 * S
        kv_pad = _pad(kv, ((0, 0), (0, S), (0, 0)))
        kf_pad = _pad(ssm_k, ((0, 0), (0, S)))
        kv_f = (lambda x, axes=(-2,-1): torch.fft.rfft2(x.float(), dim=axes))(kv_pad[:, None, :, :], axes=(1, 2))
        kf_f = (lambda x, axes=(-2,-1): torch.fft.rfft2(x.float(), dim=axes))(kf_pad[:, None, :], axes=(1, 2))
        kf_f = (lambda x, *d: x.permute(*d))(kf_f, 1, 2, 0)
        y_f = kv_f * kf_f[None, :, :, :]
        y = (lambda x, s, axes=(-2,-1): torch.fft.irfft2(x, s=s, dim=axes).to(torch.bfloat16))(y_f, (1, L), axes=(1, 2))[:, 0, :S, :]
        y = y + kv * ssm_D[None, None, :]
        return q * y

    a = _A_ref(A_x, A_wqkv, A_wo)
    b = _B_ref(B_q, B_k, B_v, B_shift_k, B_ssm_k, B_ssm_D)
    return torch.concatenate([a, b], axis=-1)
```

```cpp
#include <hip/hip_runtime.h>
#include <hip/hip_bf16.h>
#include <stdint.h>

typedef unsigned short u16;
using f32x16 = __attribute__((ext_vector_type(16))) float;
using f32x4 = __attribute__((ext_vector_type(4))) float;
using bf16x8 = __attribute__((ext_vector_type(8))) short;
using bf16x4 = __attribute__((ext_vector_type(4))) short;
using u32x4 = __attribute__((ext_vector_type(4))) unsigned;
using u32x2 = __attribute__((ext_vector_type(2))) unsigned;
typedef __bf16 bf2_t __attribute__((ext_vector_type(2)));
typedef float f2_t __attribute__((ext_vector_type(2)));
typedef short v4i16_t __attribute__((ext_vector_type(4)));
#define LDS_AS __attribute__((address_space(3)))

constexpr int SEQ = 2048, DM = 576, NH = 32;
constexpr int KVG = 10240;
constexpr int XPS = 2144;
constexpr unsigned WS_FLG = 0;
constexpr unsigned FL_P1 = 128, FL_C1 = FL_P1 + 14 * 16 * 4, FL_C2 = FL_C1 + 288 * 4, FL_O = FL_C2 + 256 * 4, FL_END = FL_O + 256 * 4;
constexpr unsigned WS_QF0 = 8192;
constexpr unsigned WS_QF1 = WS_QF0 + NH * 64 * 1024;
constexpr unsigned WS_KV = WS_QF1 + NH * SEQ * 8;
constexpr unsigned WS_KMX = WS_KV + NH * 16 * KVG;
constexpr unsigned WS_OM = WS_KMX + 32 * 5 * 32 * 4;
constexpr unsigned WS_OR = WS_OM + NH * SEQ * 32;
constexpr unsigned WS_XP = WS_OR + NH * SEQ * 4;
constexpr unsigned WS_YT = WS_XP + (DM + 1) * XPS * 2;
constexpr unsigned WS_END = WS_YT + DM * SEQ * 2;
static_assert(FL_END <= WS_QF0, "flags");
constexpr float QSCALE = 0.23570226039551584f * 1.4426950408889634f;
constexpr float SAFE_B = 50.0f;
constexpr int LDS_BYTES = 163840;
constexpr unsigned L_RING = 0;
constexpr unsigned L_C1X = 133120, L_C1M = 101888;
constexpr unsigned L_C3 = 83968;
constexpr unsigned L_ATT = 101888;
constexpr unsigned L_SYNC = 163328;
constexpr int SY_EPOCH = 0, SY_P1 = 1, SY_C1X = 2, SY_C1M = 3, SY_C2 = 4, SY_O = 5, SY_GO = 6, SY_GO2 = 7, SY_BAD = 8  , SY_CV = 16  ,
              SY_RING = 32  , SY_EPRDY = 50, SY_BC1X = 51, SY_BC1M = 52, SY_C3 = 53, SY_GO3 = 54, SY_WO = 55, SY_MB = 56  ,
              SY_CVDONE = 60, SY_ATT = 64  ;
constexpr int CV_A = 4416, CV_HB = 4 * CV_A, CV_SZ = CV_HB + 96 * 80;

__device__ __forceinline__ unsigned cvtpk(float lo, float hi) { f2_t v = {lo, hi}; bf2_t r = __builtin_convertvector(v, bf2_t); return __builtin_bit_cast(unsigned, r); }
__device__ __forceinline__ float bflo(unsigned u) { return __uint_as_float(u << 16); }
__device__ __forceinline__ float bfhi(unsigned u) { return __uint_as_float(u & 0xffff0000u); }
__device__ __forceinline__ float rbf(float x) { return bflo(cvtpk(x, x)); }
#define MFMA32(a, b, c) __builtin_amdgcn_mfma_f32_32x32x16_bf16(a, b, c, 0, 0, 0)
#define MFMA32K8(a, b, c) __builtin_amdgcn_mfma_f32_32x32x8bf16_1k(a, b, c, 0, 0, 0)
#define MFMA16(a, b, c) __builtin_amdgcn_mfma_f32_16x16x32_bf16(a, b, c, 0, 0, 0)
__device__ __forceinline__ unsigned rfl(unsigned v) { return __builtin_amdgcn_readfirstlane(v); }
__device__ __forceinline__ int lane_id() { return (int)__builtin_amdgcn_mbcnt_hi(~0u, __builtin_amdgcn_mbcnt_lo(~0u, 0u)); }
__device__ __forceinline__ void glds16(const void* g, unsigned lds_base) {
    unsigned sv;
    asm volatile("s_mov_b32 %0, m0\n\ts_mov_b32 m0, %2\n\ts_nop 0\n\tglobal_load_lds_dwordx4 %1, off\n\ts_mov_b32 m0, %0" : "=&s"(sv) : "v"(g), "s"(lds_base) : "memory");
}
__device__ __forceinline__ void glds16_sc1(const void* g, unsigned lds_base) {
    unsigned sv;
    asm volatile("s_mov_b32 %0, m0\n\ts_mov_b32 m0, %2\n\ts_nop 0\n\tglobal_load_lds_dwordx4 %1, off sc1\n\ts_mov_b32 m0, %0" : "=&s"(sv) : "v"(g), "s"(lds_base) : "memory");
}
#define ST(i)
#define STC(i)
#define STDUMP()
#define STARG
#define STPASS
#define WAIT_BAR(N) asm volatile("s_waitcnt vmcnt(" #N ") lgkmcnt(0)\n\ts_barrier" ::: "memory")
#define BAR() asm volatile("s_waitcnt lgkmcnt(0)\n\ts_barrier" ::: "memory")
#define WAITV0() asm volatile("s_waitcnt vmcnt(0)" ::: "memory")
__device__ __forceinline__ bf16x4 trread(unsigned addr) {
    return __builtin_bit_cast(bf16x4, __builtin_amdgcn_ds_read_tr16_b64_v4i16((LDS_AS v4i16_t*)(uintptr_t)addr));
}
template <class T> __device__ __forceinline__ T ldsrd(unsigned addr) { return *(const LDS_AS T*)(uintptr_t)addr; }
template <class T> __device__ __forceinline__ void ldswr(unsigned addr, T v) { *(LDS_AS T*)(uintptr_t)addr = v; }
__device__ __forceinline__ unsigned lds_vol(unsigned addr) { return *(volatile LDS_AS unsigned*)(uintptr_t)addr; }
__device__ __forceinline__ unsigned lds_add(unsigned addr) { return __hip_atomic_fetch_add((LDS_AS unsigned*)(uintptr_t)addr, 1u, __ATOMIC_RELAXED, __HIP_MEMORY_SCOPE_WORKGROUP); }
typedef __amdgpu_buffer_rsrc_t rsrc_t;
__device__ __forceinline__ u32x4 ld128(rsrc_t r, unsigned off) { return __builtin_amdgcn_raw_buffer_load_b128(r, off, 0, 16); }
__device__ __forceinline__ u32x2 ld64(rsrc_t r, unsigned off) { return __builtin_amdgcn_raw_buffer_load_b64(r, off, 0, 16); }
__device__ __forceinline__ unsigned ld32(rsrc_t r, unsigned off) { return __builtin_amdgcn_raw_buffer_load_b32(r, off, 0, 16); }
__device__ __forceinline__ void st128(rsrc_t r, unsigned off, u32x4 v) { __builtin_amdgcn_raw_buffer_store_b128(v, r, off, 0, 16); }
__device__ __forceinline__ void st64(rsrc_t r, unsigned off, u32x2 v) { __builtin_amdgcn_raw_buffer_store_b64(v, r, off, 0, 16); }
__device__ __forceinline__ void st32(rsrc_t r, unsigned off, unsigned v) { __builtin_amdgcn_raw_buffer_store_b32(v, r, off, 0, 16); }
typedef __attribute__((address_space(1))) unsigned gu32;
__device__ __forceinline__ unsigned flag_ld(const char* ws, unsigned off) { return __hip_atomic_load((gu32*)(ws + off), __ATOMIC_RELAXED, __HIP_MEMORY_SCOPE_AGENT); }
__device__ __forceinline__ void flag_st(char* ws, unsigned off, unsigned v) { __hip_atomic_store((gu32*)(ws + off), v, __ATOMIC_RELAXED, __HIP_MEMORY_SCOPE_AGENT); }
constexpr unsigned SPIN_MAX = 1u << 16, LSPIN_MAX = 1u << 20;
__device__ __forceinline__ void poll(const char* ws, unsigned off0, bool act0, unsigned off1, bool act1, unsigned epoch) {
#pragma unroll 1
    for (unsigned spins = 0; spins < SPIN_MAX; ++spins) {
        const unsigned a = act0 ? flag_ld(ws, off0) : epoch, b = act1 ? flag_ld(ws, off1) : epoch;
        if (__all(a == epoch && b == epoch)) break;
        __builtin_amdgcn_s_sleep(6);
    }
    asm volatile("" ::: "memory");
}
__device__ __forceinline__ void lds_spin(unsigned addr, unsigned target) {
#pragma unroll 1
    for (unsigned spins = 0; spins < LSPIN_MAX; ++spins) { if (rfl(lds_vol(addr)) >= target) break; }
    asm volatile("" ::: "memory");
}
__device__ __forceinline__ void lds_wait(unsigned addr, unsigned target) {
#pragma unroll 1
    for (unsigned spins = 0; spins < LSPIN_MAX; ++spins) { if (rfl(lds_vol(addr)) >= target) break; __builtin_amdgcn_s_sleep(2); }
    asm volatile("" ::: "memory");
}
__device__ __forceinline__ unsigned get_epoch(unsigned lds0) { lds_wait(lds0 + L_SYNC + SY_EPRDY * 4, 1); return rfl(lds_vol(lds0 + L_SYNC + SY_EPOCH * 4)); }
__device__ __forceinline__ void publish(char* ws, unsigned flag_off, unsigned cnt_addr, unsigned nwaves, unsigned epoch) {
    WAITV0();
    if (lane_id() == 0) { if (lds_add(cnt_addr) == nwaves - 1) flag_st(ws, flag_off, epoch); }
}
__device__ __forceinline__ void sw_barrier(unsigned addr, unsigned target) {
    asm volatile("s_waitcnt lgkmcnt(0)" ::: "memory");
    if (lane_id() == 0) lds_add(addr);
    lds_wait(addr, target);
}
__device__ __forceinline__ float half_max_nn(float v) {
#define DPPMAX(ctrl, rm) v = fmaxf(v, __builtin_bit_cast(float, __builtin_amdgcn_update_dpp(0, __builtin_bit_cast(int, v), ctrl, rm, 0xf, true)))
    DPPMAX(0x111, 0xf); DPPMAX(0x112, 0xf); DPPMAX(0x114, 0xf); DPPMAX(0x118, 0xf); DPPMAX(0x142, 0xa);
    return v;
}
__device__ __forceinline__ float wave_max_nn(float v) {
    v = half_max_nn(v); DPPMAX(0x143, 0xc);
#undef DPPMAX
    return v;
}
__device__ __forceinline__ float swap32(float v) {
    const auto r = __builtin_amdgcn_permlane32_swap(__float_as_uint(v), __float_as_uint(v), false, false);
    return __uint_as_float((lane_id() & 32) ? r[0] : r[1]);
}

__constant__ __attribute__((aligned(8))) const unsigned char P1_TAB[9 * 32] = {0, 1, 2, 3, 5, 6, 7, 8, 9, 10, 11, 12, 14, 15, 16, 17, 18, 19, 20, 21, 23, 24, 25, 26, 27, 28, 29, 30, 4, 13, 22, 31, 0, 1, 2, 3, 4, 5, 6, 7, 9, 10, 11, 12, 13, 14, 15, 16, 18, 19, 20, 21, 22, 23, 24, 25, 27, 28, 29, 30, 8, 17, 26, 31, 4, 5, 6, 7, 8, 9, 10, 11, 13, 14, 15, 16, 17, 18, 19, 20, 22, 23, 24, 25, 26, 27, 28, 29, 0, 1, 2, 3, 12, 21, 30, 31, 3, 4, 5, 6, 8, 9, 10, 11, 12, 13, 14, 15, 17, 18, 19, 20, 21, 22, 23, 24, 26, 27, 28, 29, 0, 1, 2, 7, 16, 25, 30, 31, 3, 4, 5, 6, 7, 8, 9, 10, 12, 13, 14, 15, 16, 17, 18, 19, 21, 22, 23, 24, 25, 26, 27, 28, 0, 1, 2, 11, 20, 29, 30, 31, 2, 3, 4, 5, 7, 8, 9, 10, 11, 12, 13, 14, 16, 17, 18, 19, 20, 21, 22, 23, 25, 26, 27, 28, 0, 1, 6, 15, 24, 29, 30, 31, 2, 3, 4, 5, 6, 7, 8, 9, 11, 12, 13, 14, 15, 16, 17, 18, 20, 21, 22, 23, 24, 25, 26, 27, 0, 1, 10, 19, 28, 29, 30, 31, 1, 2, 3, 4, 6, 7, 8, 9, 10, 11, 12, 13, 15, 16, 17, 18, 19, 20, 21, 22, 24, 25, 26, 27, 28, 29, 30, 31, 0, 5, 14, 23, 1, 2, 3, 4, 5, 6, 7, 8, 10, 11, 12, 13, 14, 15, 16, 17, 19, 20, 21, 22, 23, 24, 25, 26, 28, 29, 30, 31, 0, 9, 18, 27};
__constant__ __attribute__((aligned(8))) const unsigned char P1_KIND[9 * 8] = {1, 1, 1, 1, 1, 1, 1, 0, 1, 1, 1, 1, 1, 1, 1, 0, 1, 1, 1, 1, 1, 1, 0, 0, 1, 1, 1, 1, 1, 1, 0, 0, 1, 1, 1, 1, 1, 1, 0, 0, 1, 1, 1, 1, 1, 1, 0, 0, 1, 1, 1, 1, 1, 1, 0, 0, 1, 1, 1, 1, 1, 1, 1, 0, 1, 1, 1, 1, 1, 1, 1, 0};

__device__ __forceinline__ void p1_compute_qk(f32x16 (&acc)[4], const unsigned (&ax)[8], const unsigned (&aw)[4], unsigned ac) {
    unsigned t;
    asm volatile(
        "v_mov_b32 v110, 1\n\t"
        "P1SP1_%=:\n\t"
        "ds_read_b32 v109, v108 offset:0\n\t"
        "s_waitcnt lgkmcnt(0)\n\t"
        "v_readfirstlane_b32 %[t], v109\n\t"
        "s_nop 1\n\t"
        "s_cmp_ge_u32 %[t], 8\n\t"
        "s_cbranch_scc0 P1SP1_%=\n\t"
        "P1OK1_%=:\n\t"
        "ds_read_b128 v[64:67], v96 offset:0\n\t"
        "ds_read_b64_tr_b16 v[72:73], v104 offset:16384\n\t"
        "ds_read_b64_tr_b16 v[74:75], v104 offset:17408\n\t"
        "ds_read_b128 v[68:71], v96 offset:4096\n\t"
        "ds_read_b64_tr_b16 v[76:77], v105 offset:16384\n\t"
        "ds_read_b64_tr_b16 v[78:79], v105 offset:17408\n\t"
        "s_waitcnt lgkmcnt(3)\n\t"
        "v_mfma_f32_32x32x16_bf16 v[0:15], v[72:75], v[64:67], 0\n\t"
        "ds_read_b128 v[80:83], v97 offset:0\n\t"
        "ds_read_b64_tr_b16 v[88:89], v104 offset:20480\n\t"
        "s_waitcnt lgkmcnt(4)\n\t"
        "v_mfma_f32_32x32x16_bf16 v[16:31], v[72:75], v[68:71], 0\n\t"
        "ds_read_b64_tr_b16 v[90:91], v104 offset:21504\n\t"
        "s_waitcnt lgkmcnt(3)\n\t"
        "v_mfma_f32_32x32x16_bf16 v[32:47], v[76:79], v[64:67], 0\n\t"
        "ds_read_b128 v[84:87], v97 offset:4096\n\t"
        "ds_read_b64_tr_b16 v[92:93], v105 offset:20480\n\t"
        "v_mfma_f32_32x32x16_bf16 v[48:63], v[76:79], v[68:71], 0\n\t"
        "ds_read_b64_tr_b16 v[94:95], v105 offset:21504\n\t"
        "s_waitcnt lgkmcnt(3)\n\t"
        "v_mfma_f32_32x32x16_bf16 v[0:15], v[88:91], v[80:83], v[0:15]\n\t"
        "ds_read_b128 v[64:67], v98 offset:0\n\t"
        "ds_read_b64_tr_b16 v[72:73], v104 offset:24576\n\t"
        "s_waitcnt lgkmcnt(4)\n\t"
        "v_mfma_f32_32x32x16_bf16 v[16:31], v[88:91], v[84:87], v[16:31]\n\t"
        "ds_read_b64_tr_b16 v[74:75], v104 offset:25600\n\t"
        "s_waitcnt lgkmcnt(3)\n\t"
        "v_mfma_f32_32x32x16_bf16 v[32:47], v[92:95], v[80:83], v[32:47]\n\t"
        "ds_read_b128 v[68:71], v98 offset:4096\n\t"
        "ds_read_b64_tr_b16 v[76:77], v105 offset:24576\n\t"
        "v_mfma_f32_32x32x16_bf16 v[48:63], v[92:95], v[84:87], v[48:63]\n\t"
        "ds_read_b64_tr_b16 v[78:79], v105 offset:25600\n\t"
        "s_waitcnt lgkmcnt(3)\n\t"
        "v_mfma_f32_32x32x16_bf16 v[0:15], v[72:75], v[64:67], v[0:15]\n\t"
        "ds_read_b128 v[80:83], v99 offset:0\n\t"
        "ds_read_b64_tr_b16 v[88:89], v104 offset:28672\n\t"
        "s_waitcnt lgkmcnt(4)\n\t"
        "v_mfma_f32_32x32x16_bf16 v[16:31], v[72:75], v[68:71], v[16:31]\n\t"
        "ds_read_b64_tr_b16 v[90:91], v104 offset:29696\n\t"
        "ds_read_b32 v109, v108 offset:4\n\t"
        "s_waitcnt lgkmcnt(4)\n\t"
        "v_mfma_f32_32x32x16_bf16 v[32:47], v[76:79], v[64:67], v[32:47]\n\t"
        "ds_read_b128 v[84:87], v99 offset:4096\n\t"
        "ds_read_b64_tr_b16 v[92:93], v105 offset:28672\n\t"
        "v_mfma_f32_32x32x16_bf16 v[48:63], v[76:79], v[68:71], v[48:63]\n\t"
        "ds_read_b64_tr_b16 v[94:95], v105 offset:29696\n\t"
        "s_waitcnt lgkmcnt(3)\n\t"
        "v_readfirstlane_b32 %[t], v109\n\t"
        "s_nop 1\n\t"
        "s_cmp_ge_u32 %[t], 8\n\t"
        "s_cbranch_scc1 P1OK2_%=\n\t"
        "P1SP2_%=:\n\t"
        "ds_read_b32 v109, v108 offset:4\n\t"
        "s_waitcnt lgkmcnt(0)\n\t"
        "v_readfirstlane_b32 %[t], v109\n\t"
        "s_nop 1\n\t"
        "s_cmp_ge_u32 %[t], 8\n\t"
        "s_cbranch_scc0 P1SP2_%=\n\t"
        "P1OK2_%=:\n\t"
        "v_mfma_f32_32x32x16_bf16 v[0:15], v[88:91], v[80:83], v[0:15]\n\t"
        "ds_read_b128 v[64:67], v96 offset:32768\n\t"
        "ds_read_b64_tr_b16 v[72:73], v104 offset:49152\n\t"
        "s_waitcnt lgkmcnt(4)\n\t"
        "v_mfma_f32_32x32x16_bf16 v[16:31], v[88:91], v[84:87], v[16:31]\n\t"
        "ds_read_b64_tr_b16 v[74:75], v104 offset:50176\n\t"
        "s_waitcnt lgkmcnt(3)\n\t"
        "s_mov_b64 exec, 1\n\t"
        "ds_add_u32 v108, v110 offset:36\n\t"
        "s_mov_b64 exec, -1\n\t"
        "v_mfma_f32_32x32x16_bf16 v[32:47], v[92:95], v[80:83], v[32:47]\n\t"
        "ds_read_b128 v[68:71], v96 offset:36864\n\t"
        "ds_read_b64_tr_b16 v[76:77], v105 offset:49152\n\t"
        "v_mfma_f32_32x32x16_bf16 v[48:63], v[92:95], v[84:87], v[48:63]\n\t"
        "ds_read_b64_tr_b16 v[78:79], v105 offset:50176\n\t"
        "s_waitcnt lgkmcnt(3)\n\t"
        "v_mfma_f32_32x32x16_bf16 v[0:15], v[72:75], v[64:67], v[0:15]\n\t"
        "ds_read_b128 v[80:83], v97 offset:32768\n\t"
        "ds_read_b64_tr_b16 v[88:89], v104 offset:53248\n\t"
        "s_waitcnt lgkmcnt(4)\n\t"
        "v_mfma_f32_32x32x16_bf16 v[16:31], v[72:75], v[68:71], v[16:31]\n\t"
        "ds_read_b64_tr_b16 v[90:91], v104 offset:54272\n\t"
        "s_waitcnt lgkmcnt(3)\n\t"
        "v_mfma_f32_32x32x16_bf16 v[32:47], v[76:79], v[64:67], v[32:47]\n\t"
        "ds_read_b128 v[84:87], v97 offset:36864\n\t"
        "ds_read_b64_tr_b16 v[92:93], v105 offset:53248\n\t"
        "v_mfma_f32_32x32x16_bf16 v[48:63], v[76:79], v[68:71], v[48:63]\n\t"
        "ds_read_b64_tr_b16 v[94:95], v105 offset:54272\n\t"
        "s_waitcnt lgkmcnt(3)\n\t"
        "v_mfma_f32_32x32x16_bf16 v[0:15], v[88:91], v[80:83], v[0:15]\n\t"
        "ds_read_b128 v[64:67], v98 offset:32768\n\t"
        "ds_read_b64_tr_b16 v[72:73], v104 offset:57344\n\t"
        "s_waitcnt lgkmcnt(4)\n\t"
        "v_mfma_f32_32x32x16_bf16 v[16:31], v[88:91], v[84:87], v[16:31]\n\t"
        "ds_read_b64_tr_b16 v[74:75], v104 offset:58368\n\t"
        "s_waitcnt lgkmcnt(3)\n\t"
        "v_mfma_f32_32x32x16_bf16 v[32:47], v[92:95], v[80:83], v[32:47]\n\t"
        "ds_read_b128 v[68:71], v98 offset:36864\n\t"
        "ds_read_b64_tr_b16 v[76:77], v105 offset:57344\n\t"
        "v_mfma_f32_32x32x16_bf16 v[48:63], v[92:95], v[84:87], v[48:63]\n\t"
        "ds_read_b64_tr_b16 v[78:79], v105 offset:58368\n\t"
        "s_waitcnt lgkmcnt(3)\n\t"
        "v_mfma_f32_32x32x16_bf16 v[0:15], v[72:75], v[64:67], v[0:15]\n\t"
        "ds_read_b128 v[80:83], v99 offset:32768\n\t"
        "ds_read_b64_tr_b16 v[88:89], v104 offset:61440\n\t"
        "s_waitcnt lgkmcnt(4)\n\t"
        "v_mfma_f32_32x32x16_bf16 v[16:31], v[72:75], v[68:71], v[16:31]\n\t"
        "ds_read_b64_tr_b16 v[90:91], v104 offset:62464\n\t"
        "ds_read_b32 v109, v108 offset:8\n\t"
        "s_waitcnt lgkmcnt(4)\n\t"
        "v_mfma_f32_32x32x16_bf16 v[32:47], v[76:79], v[64:67], v[32:47]\n\t"
        "ds_read_b128 v[84:87], v99 offset:36864\n\t"
        "ds_read_b64_tr_b16 v[92:93], v105 offset:61440\n\t"
        "v_mfma_f32_32x32x16_bf16 v[48:63], v[76:79], v[68:71], v[48:63]\n\t"
        "ds_read_b64_tr_b16 v[94:95], v105 offset:62464\n\t"
        "s_waitcnt lgkmcnt(3)\n\t"
        "v_readfirstlane_b32 %[t], v109\n\t"
        "s_nop 1\n\t"
        "s_cmp_ge_u32 %[t], 8\n\t"
        "s_cbranch_scc1 P1OK3_%=\n\t"
        "P1SP3_%=:\n\t"
        "ds_read_b32 v109, v108 offset:8\n\t"
        "s_waitcnt lgkmcnt(0)\n\t"
        "v_readfirstlane_b32 %[t], v109\n\t"
        "s_nop 1\n\t"
        "s_cmp_ge_u32 %[t], 8\n\t"
        "s_cbranch_scc0 P1SP3_%=\n\t"
        "P1OK3_%=:\n\t"
        "v_mfma_f32_32x32x16_bf16 v[0:15], v[88:91], v[80:83], v[0:15]\n\t"
        "ds_read_b128 v[64:67], v100 offset:0\n\t"
        "ds_read_b64_tr_b16 v[72:73], v106 offset:16384\n\t"
        "s_waitcnt lgkmcnt(4)\n\t"
        "v_mfma_f32_32x32x16_bf16 v[16:31], v[88:91], v[84:87], v[16:31]\n\t"
        "ds_read_b64_tr_b16 v[74:75], v106 offset:17408\n\t"
        "s_waitcnt lgkmcnt(3)\n\t"
        "s_mov_b64 exec, 1\n\t"
        "ds_add_u32 v108, v110 offset:40\n\t"
        "s_mov_b64 exec, -1\n\t"
        "v_mfma_f32_32x32x16_bf16 v[32:47], v[92:95], v[80:83], v[32:47]\n\t"
        "ds_read_b128 v[68:71], v100 offset:4096\n\t"
        "ds_read_b64_tr_b16 v[76:77], v107 offset:16384\n\t"
        "v_mfma_f32_32x32x16_bf16 v[48:63], v[92:95], v[84:87], v[48:63]\n\t"
        "ds_read_b64_tr_b16 v[78:79], v107 offset:17408\n\t"
        "s_waitcnt lgkmcnt(3)\n\t"
        "v_mfma_f32_32x32x16_bf16 v[0:15], v[72:75], v[64:67], v[0:15]\n\t"
        "ds_read_b128 v[80:83], v101 offset:0\n\t"
        "ds_read_b64_tr_b16 v[88:89], v106 offset:20480\n\t"
        "s_waitcnt lgkmcnt(4)\n\t"
        "v_mfma_f32_32x32x16_bf16 v[16:31], v[72:75], v[68:71], v[16:31]\n\t"
        "ds_read_b64_tr_b16 v[90:91], v106 offset:21504\n\t"
        "s_waitcnt lgkmcnt(3)\n\t"
        "v_mfma_f32_32x32x16_bf16 v[32:47], v[76:79], v[64:67], v[32:47]\n\t"
        "ds_read_b128 v[84:87], v101 offset:4096\n\t"
        "ds_read_b64_tr_b16 v[92:93], v107 offset:20480\n\t"
        "v_mfma_f32_32x32x16_bf16 v[48:63], v[76:79], v[68:71], v[48:63]\n\t"
        "ds_read_b64_tr_b16 v[94:95], v107 offset:21504\n\t"
        "s_waitcnt lgkmcnt(3)\n\t"
        "v_mfma_f32_32x32x16_bf16 v[0:15], v[88:91], v[80:83], v[0:15]\n\t"
        "ds_read_b128 v[64:67], v102 offset:0\n\t"
        "ds_read_b64_tr_b16 v[72:73], v106 offset:24576\n\t"
        "s_waitcnt lgkmcnt(4)\n\t"
        "v_mfma_f32_32x32x16_bf16 v[16:31], v[88:91], v[84:87], v[16:31]\n\t"
        "ds_read_b64_tr_b16 v[74:75], v106 offset:25600\n\t"
        "s_waitcnt lgkmcnt(3)\n\t"
        "v_mfma_f32_32x32x16_bf16 v[32:47], v[92:95], v[80:83], v[32:47]\n\t"
        "ds_read_b128 v[68:71], v102 offset:4096\n\t"
        "ds_read_b64_tr_b16 v[76:77], v107 offset:24576\n\t"
        "v_mfma_f32_32x32x16_bf16 v[48:63], v[92:95], v[84:87], v[48:63]\n\t"
        "ds_read_b64_tr_b16 v[78:79], v107 offset:25600\n\t"
        "s_waitcnt lgkmcnt(3)\n\t"
        "v_mfma_f32_32x32x16_bf16 v[0:15], v[72:75], v[64:67], v[0:15]\n\t"
        "ds_read_b128 v[80:83], v103 offset:0\n\t"
        "ds_read_b64_tr_b16 v[88:89], v106 offset:28672\n\t"
        "s_waitcnt lgkmcnt(4)\n\t"
        "v_mfma_f32_32x32x16_bf16 v[16:31], v[72:75], v[68:71], v[16:31]\n\t"
        "ds_read_b64_tr_b16 v[90:91], v106 offset:29696\n\t"
        "ds_read_b32 v109, v108 offset:12\n\t"
        "s_waitcnt lgkmcnt(4)\n\t"
        "v_mfma_f32_32x32x16_bf16 v[32:47], v[76:79], v[64:67], v[32:47]\n\t"
        "ds_read_b128 v[84:87], v103 offset:4096\n\t"
        "ds_read_b64_tr_b16 v[92:93], v107 offset:28672\n\t"
        "v_mfma_f32_32x32x16_bf16 v[48:63], v[76:79], v[68:71], v[48:63]\n\t"
        "ds_read_b64_tr_b16 v[94:95], v107 offset:29696\n\t"
        "s_waitcnt lgkmcnt(3)\n\t"
        "v_readfirstlane_b32 %[t], v109\n\t"
        "s_nop 1\n\t"
        "s_cmp_ge_u32 %[t], 8\n\t"
        "s_cbranch_scc1 P1OK4_%=\n\t"
        "P1SP4_%=:\n\t"
        "ds_read_b32 v109, v108 offset:12\n\t"
        "s_waitcnt lgkmcnt(0)\n\t"
        "v_readfirstlane_b32 %[t], v109\n\t"
        "s_nop 1\n\t"
        "s_cmp_ge_u32 %[t], 8\n\t"
        "s_cbranch_scc0 P1SP4_%=\n\t"
        "P1OK4_%=:\n\t"
        "v_mfma_f32_32x32x16_bf16 v[0:15], v[88:91], v[80:83], v[0:15]\n\t"
        "ds_read_b128 v[64:67], v100 offset:32768\n\t"
        "ds_read_b64_tr_b16 v[72:73], v106 offset:49152\n\t"
        "s_waitcnt lgkmcnt(4)\n\t"
        "v_mfma_f32_32x32x16_bf16 v[16:31], v[88:91], v[84:87], v[16:31]\n\t"
        "ds_read_b64_tr_b16 v[74:75], v106 offset:50176\n\t"
        "s_waitcnt lgkmcnt(3)\n\t"
        "s_mov_b64 exec, 1\n\t"
        "ds_add_u32 v108, v110 offset:44\n\t"
        "s_mov_b64 exec, -1\n\t"
        "v_mfma_f32_32x32x16_bf16 v[32:47], v[92:95], v[80:83], v[32:47]\n\t"
        "ds_read_b128 v[68:71], v100 offset:36864\n\t"
        "ds_read_b64_tr_b16 v[76:77], v107 offset:49152\n\t"
        "v_mfma_f32_32x32x16_bf16 v[48:63], v[92:95], v[84:87], v[48:63]\n\t"
        "ds_read_b64_tr_b16 v[78:79], v107 offset:50176\n\t"
        "s_waitcnt lgkmcnt(3)\n\t"
        "v_mfma_f32_32x32x16_bf16 v[0:15], v[72:75], v[64:67], v[0:15]\n\t"
        "ds_read_b128 v[80:83], v101 offset:32768\n\t"
        "ds_read_b64_tr_b16 v[88:89], v106 offset:53248\n\t"
        "s_waitcnt lgkmcnt(4)\n\t"
        "v_mfma_f32_32x32x16_bf16 v[16:31], v[72:75], v[68:71], v[16:31]\n\t"
        "ds_read_b64_tr_b16 v[90:91], v106 offset:54272\n\t"
        "s_waitcnt lgkmcnt(3)\n\t"
        "v_mfma_f32_32x32x16_bf16 v[32:47], v[76:79], v[64:67], v[32:47]\n\t"
        "ds_read_b128 v[84:87], v101 offset:36864\n\t"
        "ds_read_b64_tr_b16 v[92:93], v107 offset:53248\n\t"
        "v_mfma_f32_32x32x16_bf16 v[48:63], v[76:79], v[68:71], v[48:63]\n\t"
        "ds_read_b64_tr_b16 v[94:95], v107 offset:54272\n\t"
        "s_waitcnt lgkmcnt(3)\n\t"
        "v_mfma_f32_32x32x16_bf16 v[0:15], v[88:91], v[80:83], v[0:15]\n\t"
        "ds_read_b128 v[64:67], v102 offset:32768\n\t"
        "ds_read_b64_tr_b16 v[72:73], v106 offset:57344\n\t"
        "s_waitcnt lgkmcnt(4)\n\t"
        "v_mfma_f32_32x32x16_bf16 v[16:31], v[88:91], v[84:87], v[16:31]\n\t"
        "ds_read_b64_tr_b16 v[74:75], v106 offset:58368\n\t"
        "s_waitcnt lgkmcnt(3)\n\t"
        "v_mfma_f32_32x32x16_bf16 v[32:47], v[92:95], v[80:83], v[32:47]\n\t"
        "ds_read_b128 v[68:71], v102 offset:36864\n\t"
        "ds_read_b64_tr_b16 v[76:77], v107 offset:57344\n\t"
        "v_mfma_f32_32x32x16_bf16 v[48:63], v[92:95], v[84:87], v[48:63]\n\t"
        "ds_read_b64_tr_b16 v[78:79], v107 offset:58368\n\t"
        "s_waitcnt lgkmcnt(3)\n\t"
        "v_mfma_f32_32x32x16_bf16 v[0:15], v[72:75], v[64:67], v[0:15]\n\t"
        "ds_read_b128 v[80:83], v103 offset:32768\n\t"
        "ds_read_b64_tr_b16 v[88:89], v106 offset:61440\n\t"
        "s_waitcnt lgkmcnt(4)\n\t"
        "v_mfma_f32_32x32x16_bf16 v[16:31], v[72:75], v[68:71], v[16:31]\n\t"
        "ds_read_b64_tr_b16 v[90:91], v106 offset:62464\n\t"
        "ds_read_b32 v109, v108 offset:16\n\t"
        "s_waitcnt lgkmcnt(4)\n\t"
        "v_mfma_f32_32x32x16_bf16 v[32:47], v[76:79], v[64:67], v[32:47]\n\t"
        "ds_read_b128 v[84:87], v103 offset:36864\n\t"
        "ds_read_b64_tr_b16 v[92:93], v107 offset:61440\n\t"
        "v_mfma_f32_32x32x16_bf16 v[48:63], v[76:79], v[68:71], v[48:63]\n\t"
        "ds_read_b64_tr_b16 v[94:95], v107 offset:62464\n\t"
        "s_waitcnt lgkmcnt(3)\n\t"
        "v_readfirstlane_b32 %[t], v109\n\t"
        "s_nop 1\n\t"
        "s_cmp_ge_u32 %[t], 8\n\t"
        "s_cbranch_scc1 P1OK5_%=\n\t"
        "P1SP5_%=:\n\t"
        "ds_read_b32 v109, v108 offset:16\n\t"
        "s_waitcnt lgkmcnt(0)\n\t"
        "v_readfirstlane_b32 %[t], v109\n\t"
        "s_nop 1\n\t"
        "s_cmp_ge_u32 %[t], 8\n\t"
        "s_cbranch_scc0 P1SP5_%=\n\t"
        "P1OK5_%=:\n\t"
        "v_mfma_f32_32x32x16_bf16 v[0:15], v[88:91], v[80:83], v[0:15]\n\t"
        "ds_read_b128 v[64:67], v96 offset:0\n\t"
        "ds_read_b64_tr_b16 v[72:73], v104 offset:16384\n\t"
        "s_waitcnt lgkmcnt(4)\n\t"
        "v_mfma_f32_32x32x16_bf16 v[16:31], v[88:91], v[84:87], v[16:31]\n\t"
        "ds_read_b64_tr_b16 v[74:75], v104 offset:17408\n\t"
        "s_waitcnt lgkmcnt(3)\n\t"
        "s_mov_b64 exec, 1\n\t"
        "ds_add_u32 v108, v110 offset:48\n\t"
        "s_mov_b64 exec, -1\n\t"
        "v_mfma_f32_32x32x16_bf16 v[32:47], v[92:95], v[80:83], v[32:47]\n\t"
        "ds_read_b128 v[68:71], v96 offset:4096\n\t"
        "ds_read_b64_tr_b16 v[76:77], v105 offset:16384\n\t"
        "v_mfma_f32_32x32x16_bf16 v[48:63], v[92:95], v[84:87], v[48:63]\n\t"
        "ds_read_b64_tr_b16 v[78:79], v105 offset:17408\n\t"
        "s_waitcnt lgkmcnt(3)\n\t"
        "v_mfma_f32_32x32x16_bf16 v[0:15], v[72:75], v[64:67], v[0:15]\n\t"
        "ds_read_b128 v[80:83], v97 offset:0\n\t"
        "ds_read_b64_tr_b16 v[88:89], v104 offset:20480\n\t"
        "s_waitcnt lgkmcnt(4)\n\t"
        "v_mfma_f32_32x32x16_bf16 v[16:31], v[72:75], v[68:71], v[16:31]\n\t"
        "ds_read_b64_tr_b16 v[90:91], v104 offset:21504\n\t"
        "s_waitcnt lgkmcnt(3)\n\t"
        "v_mfma_f32_32x32x16_bf16 v[32:47], v[76:79], v[64:67], v[32:47]\n\t"
        "ds_read_b128 v[84:87], v97 offset:4096\n\t"
        "ds_read_b64_tr_b16 v[92:93], v105 offset:20480\n\t"
        "v_mfma_f32_32x32x16_bf16 v[48:63], v[76:79], v[68:71], v[48:63]\n\t"
        "ds_read_b64_tr_b16 v[94:95], v105 offset:21504\n\t"
        "s_waitcnt lgkmcnt(3)\n\t"
        "v_mfma_f32_32x32x16_bf16 v[0:15], v[88:91], v[80:83], v[0:15]\n\t"
        "ds_read_b128 v[64:67], v98 offset:0\n\t"
        "ds_read_b64_tr_b16 v[72:73], v104 offset:24576\n\t"
        "s_waitcnt lgkmcnt(4)\n\t"
        "v_mfma_f32_32x32x16_bf16 v[16:31], v[88:91], v[84:87], v[16:31]\n\t"
        "ds_read_b64_tr_b16 v[74:75], v104 offset:25600\n\t"
        "s_waitcnt lgkmcnt(3)\n\t"
        "v_mfma_f32_32x32x16_bf16 v[32:47], v[92:95], v[80:83], v[32:47]\n\t"
        "ds_read_b128 v[68:71], v98 offset:4096\n\t"
        "ds_read_b64_tr_b16 v[76:77], v105 offset:24576\n\t"
        "v_mfma_f32_32x32x16_bf16 v[48:63], v[92:95], v[84:87], v[48:63]\n\t"
        "ds_read_b64_tr_b16 v[78:79], v105 offset:25600\n\t"
        "s_waitcnt lgkmcnt(3)\n\t"
        "v_mfma_f32_32x32x16_bf16 v[0:15], v[72:75], v[64:67], v[0:15]\n\t"
        "ds_read_b128 v[80:83], v99 offset:0\n\t"
        "ds_read_b64_tr_b16 v[88:89], v104 offset:28672\n\t"
        "s_waitcnt lgkmcnt(4)\n\t"
        "v_mfma_f32_32x32x16_bf16 v[16:31], v[72:75], v[68:71], v[16:31]\n\t"
        "ds_read_b64_tr_b16 v[90:91], v104 offset:29696\n\t"
        "ds_read_b32 v109, v108 offset:20\n\t"
        "s_waitcnt lgkmcnt(4)\n\t"
        "v_mfma_f32_32x32x16_bf16 v[32:47], v[76:79], v[64:67], v[32:47]\n\t"
        "ds_read_b128 v[84:87], v99 offset:4096\n\t"
        "ds_read_b64_tr_b16 v[92:93], v105 offset:28672\n\t"
        "v_mfma_f32_32x32x16_bf16 v[48:63], v[76:79], v[68:71], v[48:63]\n\t"
        "ds_read_b64_tr_b16 v[94:95], v105 offset:29696\n\t"
        "s_waitcnt lgkmcnt(3)\n\t"
        "v_readfirstlane_b32 %[t], v109\n\t"
        "s_nop 1\n\t"
        "s_cmp_ge_u32 %[t], 8\n\t"
        "s_cbranch_scc1 P1OK6_%=\n\t"
        "P1SP6_%=:\n\t"
        "ds_read_b32 v109, v108 offset:20\n\t"
        "s_waitcnt lgkmcnt(0)\n\t"
        "v_readfirstlane_b32 %[t], v109\n\t"
        "s_nop 1\n\t"
        "s_cmp_ge_u32 %[t], 8\n\t"
        "s_cbranch_scc0 P1SP6_%=\n\t"
        "P1OK6_%=:\n\t"
        "v_mfma_f32_32x32x16_bf16 v[0:15], v[88:91], v[80:83], v[0:15]\n\t"
        "ds_read_b128 v[64:67], v96 offset:32768\n\t"
        "ds_read_b64_tr_b16 v[72:73], v104 offset:49152\n\t"
        "s_waitcnt lgkmcnt(4)\n\t"
        "v_mfma_f32_32x32x16_bf16 v[16:31], v[88:91], v[84:87], v[16:31]\n\t"
        "ds_read_b64_tr_b16 v[74:75], v104 offset:50176\n\t"
        "s_waitcnt lgkmcnt(3)\n\t"
        "s_mov_b64 exec, 1\n\t"
        "ds_add_u32 v108, v110 offset:52\n\t"
        "s_mov_b64 exec, -1\n\t"
        "v_mfma_f32_32x32x16_bf16 v[32:47], v[92:95], v[80:83], v[32:47]\n\t"
        "ds_read_b128 v[68:71], v96 offset:36864\n\t"
        "ds_read_b64_tr_b16 v[76:77], v105 offset:49152\n\t"
        "v_mfma_f32_32x32x16_bf16 v[48:63], v[92:95], v[84:87], v[48:63]\n\t"
        "ds_read_b64_tr_b16 v[78:79], v105 offset:50176\n\t"
        "s_waitcnt lgkmcnt(3)\n\t"
        "v_mfma_f32_32x32x16_bf16 v[0:15], v[72:75], v[64:67], v[0:15]\n\t"
        "ds_read_b128 v[80:83], v97 offset:32768\n\t"
        "ds_read_b64_tr_b16 v[88:89], v104 offset:53248\n\t"
        "s_waitcnt lgkmcnt(4)\n\t"
        "v_mfma_f32_32x32x16_bf16 v[16:31], v[72:75], v[68:71], v[16:31]\n\t"
        "ds_read_b64_tr_b16 v[90:91], v104 offset:54272\n\t"
        "s_waitcnt lgkmcnt(3)\n\t"
        "v_mfma_f32_32x32x16_bf16 v[32:47], v[76:79], v[64:67], v[32:47]\n\t"
        "ds_read_b128 v[84:87], v97 offset:36864\n\t"
        "ds_read_b64_tr_b16 v[92:93], v105 offset:53248\n\t"
        "v_mfma_f32_32x32x16_bf16 v[48:63], v[76:79], v[68:71], v[48:63]\n\t"
        "ds_read_b64_tr_b16 v[94:95], v105 offset:54272\n\t"
        "s_waitcnt lgkmcnt(3)\n\t"
        "v_mfma_f32_32x32x16_bf16 v[0:15], v[88:91], v[80:83], v[0:15]\n\t"
        "ds_read_b128 v[64:67], v98 offset:32768\n\t"
        "ds_read_b64_tr_b16 v[72:73], v104 offset:57344\n\t"
        "s_waitcnt lgkmcnt(4)\n\t"
        "v_mfma_f32_32x32x16_bf16 v[16:31], v[88:91], v[84:87], v[16:31]\n\t"
        "ds_read_b64_tr_b16 v[74:75], v104 offset:58368\n\t"
        "s_waitcnt lgkmcnt(3)\n\t"
        "v_mfma_f32_32x32x16_bf16 v[32:47], v[92:95], v[80:83], v[32:47]\n\t"
        "ds_read_b128 v[68:71], v98 offset:36864\n\t"
        "ds_read_b64_tr_b16 v[76:77], v105 offset:57344\n\t"
        "v_mfma_f32_32x32x16_bf16 v[48:63], v[92:95], v[84:87], v[48:63]\n\t"
        "ds_read_b64_tr_b16 v[78:79], v105 offset:58368\n\t"
        "s_waitcnt lgkmcnt(3)\n\t"
        "v_mfma_f32_32x32x16_bf16 v[0:15], v[72:75], v[64:67], v[0:15]\n\t"
        "ds_read_b128 v[80:83], v99 offset:32768\n\t"
        "ds_read_b64_tr_b16 v[88:89], v104 offset:61440\n\t"
        "s_waitcnt lgkmcnt(4)\n\t"
        "v_mfma_f32_32x32x16_bf16 v[16:31], v[72:75], v[68:71], v[16:31]\n\t"
        "ds_read_b64_tr_b16 v[90:91], v104 offset:62464\n\t"
        "ds_read_b32 v109, v108 offset:24\n\t"
        "s_waitcnt lgkmcnt(4)\n\t"
        "v_mfma_f32_32x32x16_bf16 v[32:47], v[76:79], v[64:67], v[32:47]\n\t"
        "ds_read_b128 v[84:87], v99 offset:36864\n\t"
        "ds_read_b64_tr_b16 v[92:93], v105 offset:61440\n\t"
        "v_mfma_f32_32x32x16_bf16 v[48:63], v[76:79], v[68:71], v[48:63]\n\t"
        "ds_read_b64_tr_b16 v[94:95], v105 offset:62464\n\t"
        "s_waitcnt lgkmcnt(3)\n\t"
        "v_readfirstlane_b32 %[t], v109\n\t"
        "s_nop 1\n\t"
        "s_cmp_ge_u32 %[t], 8\n\t"
        "s_cbranch_scc1 P1OK7_%=\n\t"
        "P1SP7_%=:\n\t"
        "ds_read_b32 v109, v108 offset:24\n\t"
        "s_waitcnt lgkmcnt(0)\n\t"
        "v_readfirstlane_b32 %[t], v109\n\t"
        "s_nop 1\n\t"
        "s_cmp_ge_u32 %[t], 8\n\t"
        "s_cbranch_scc0 P1SP7_%=\n\t"
        "P1OK7_%=:\n\t"
        "v_mfma_f32_32x32x16_bf16 v[0:15], v[88:91], v[80:83], v[0:15]\n\t"
        "ds_read_b128 v[64:67], v100 offset:0\n\t"
        "ds_read_b64_tr_b16 v[72:73], v106 offset:16384\n\t"
        "s_waitcnt lgkmcnt(4)\n\t"
        "v_mfma_f32_32x32x16_bf16 v[16:31], v[88:91], v[84:87], v[16:31]\n\t"
        "ds_read_b64_tr_b16 v[74:75], v106 offset:17408\n\t"
        "s_waitcnt lgkmcnt(3)\n\t"
        "v_mfma_f32_32x32x16_bf16 v[32:47], v[92:95], v[80:83], v[32:47]\n\t"
        "ds_read_b128 v[68:71], v100 offset:4096\n\t"
        "ds_read_b64_tr_b16 v[76:77], v107 offset:16384\n\t"
        "v_mfma_f32_32x32x16_bf16 v[48:63], v[92:95], v[84:87], v[48:63]\n\t"
        "ds_read_b64_tr_b16 v[78:79], v107 offset:17408\n\t"
        "s_waitcnt lgkmcnt(3)\n\t"
        "v_mfma_f32_32x32x16_bf16 v[0:15], v[72:75], v[64:67], v[0:15]\n\t"
        "ds_read_b128 v[80:83], v101 offset:0\n\t"
        "ds_read_b64_tr_b16 v[88:89], v106 offset:20480\n\t"
        "s_waitcnt lgkmcnt(4)\n\t"
        "v_mfma_f32_32x32x16_bf16 v[16:31], v[72:75], v[68:71], v[16:31]\n\t"
        "ds_read_b64_tr_b16 v[90:91], v106 offset:21504\n\t"
        "s_waitcnt lgkmcnt(3)\n\t"
        "v_mfma_f32_32x32x16_bf16 v[32:47], v[76:79], v[64:67], v[32:47]\n\t"
        "ds_read_b128 v[84:87], v101 offset:4096\n\t"
        "ds_read_b64_tr_b16 v[92:93], v107 offset:20480\n\t"
        "v_mfma_f32_32x32x16_bf16 v[48:63], v[76:79], v[68:71], v[48:63]\n\t"
        "ds_read_b64_tr_b16 v[94:95], v107 offset:21504\n\t"
        "s_waitcnt lgkmcnt(3)\n\t"
        "v_mfma_f32_32x32x16_bf16 v[0:15], v[88:91], v[80:83], v[0:15]\n\t"
        "ds_read_b128 v[64:67], v102 offset:0\n\t"
        "ds_read_b64_tr_b16 v[72:73], v106 offset:24576\n\t"
        "s_waitcnt lgkmcnt(4)\n\t"
        "v_mfma_f32_32x32x16_bf16 v[16:31], v[88:91], v[84:87], v[16:31]\n\t"
        "ds_read_b64_tr_b16 v[74:75], v106 offset:25600\n\t"
        "s_waitcnt lgkmcnt(3)\n\t"
        "v_mfma_f32_32x32x16_bf16 v[32:47], v[92:95], v[80:83], v[32:47]\n\t"
        "ds_read_b128 v[68:71], v102 offset:4096\n\t"
        "ds_read_b64_tr_b16 v[76:77], v107 offset:24576\n\t"
        "v_mfma_f32_32x32x16_bf16 v[48:63], v[92:95], v[84:87], v[48:63]\n\t"
        "ds_read_b64_tr_b16 v[78:79], v107 offset:25600\n\t"
        "s_waitcnt lgkmcnt(3)\n\t"
        "v_mfma_f32_32x32x16_bf16 v[0:15], v[72:75], v[64:67], v[0:15]\n\t"
        "ds_read_b128 v[80:83], v103 offset:0\n\t"
        "ds_read_b64_tr_b16 v[88:89], v106 offset:28672\n\t"
        "s_waitcnt lgkmcnt(4)\n\t"
        "v_mfma_f32_32x32x16_bf16 v[16:31], v[72:75], v[68:71], v[16:31]\n\t"
        "ds_read_b64_tr_b16 v[90:91], v106 offset:29696\n\t"
        "ds_read_b32 v109, v108 offset:28\n\t"
        "s_waitcnt lgkmcnt(4)\n\t"
        "v_mfma_f32_32x32x16_bf16 v[32:47], v[76:79], v[64:67], v[32:47]\n\t"
        "ds_read_b128 v[84:87], v103 offset:4096\n\t"
        "ds_read_b64_tr_b16 v[92:93], v107 offset:28672\n\t"
        "v_mfma_f32_32x32x16_bf16 v[48:63], v[76:79], v[68:71], v[48:63]\n\t"
        "ds_read_b64_tr_b16 v[94:95], v107 offset:29696\n\t"
        "s_waitcnt lgkmcnt(3)\n\t"
        "v_readfirstlane_b32 %[t], v109\n\t"
        "s_nop 1\n\t"
        "s_cmp_ge_u32 %[t], 8\n\t"
        "s_cbranch_scc1 P1OK8_%=\n\t"
        "P1SP8_%=:\n\t"
        "ds_read_b32 v109, v108 offset:28\n\t"
        "s_waitcnt lgkmcnt(0)\n\t"
        "v_readfirstlane_b32 %[t], v109\n\t"
        "s_nop 1\n\t"
        "s_cmp_ge_u32 %[t], 8\n\t"
        "s_cbranch_scc0 P1SP8_%=\n\t"
        "P1OK8_%=:\n\t"
        "v_mfma_f32_32x32x16_bf16 v[0:15], v[88:91], v[80:83], v[0:15]\n\t"
        "ds_read_b128 v[64:67], v100 offset:32768\n\t"
        "ds_read_b64_tr_b16 v[72:73], v106 offset:49152\n\t"
        "s_waitcnt lgkmcnt(4)\n\t"
        "v_mfma_f32_32x32x16_bf16 v[16:31], v[88:91], v[84:87], v[16:31]\n\t"
        "ds_read_b64_tr_b16 v[74:75], v106 offset:50176\n\t"
        "s_waitcnt lgkmcnt(3)\n\t"
        "v_mfma_f32_32x32x16_bf16 v[32:47], v[92:95], v[80:83], v[32:47]\n\t"
        "ds_read_b128 v[68:71], v100 offset:36864\n\t"
        "ds_read_b64_tr_b16 v[76:77], v107 offset:49152\n\t"
        "v_mfma_f32_32x32x16_bf16 v[48:63], v[92:95], v[84:87], v[48:63]\n\t"
        "ds_read_b64_tr_b16 v[78:79], v107 offset:50176\n\t"
        "s_waitcnt lgkmcnt(3)\n\t"
        "v_mfma_f32_32x32x16_bf16 v[0:15], v[72:75], v[64:67], v[0:15]\n\t"
        "ds_read_b128 v[80:83], v101 offset:32768\n\t"
        "ds_read_b64_tr_b16 v[88:89], v106 offset:53248\n\t"
        "s_waitcnt lgkmcnt(4)\n\t"
        "v_mfma_f32_32x32x16_bf16 v[16:31], v[72:75], v[68:71], v[16:31]\n\t"
        "ds_read_b64_tr_b16 v[90:91], v106 offset:54272\n\t"
        "s_waitcnt lgkmcnt(3)\n\t"
        "v_mfma_f32_32x32x16_bf16 v[32:47], v[76:79], v[64:67], v[32:47]\n\t"
        "ds_read_b128 v[84:87], v101 offset:36864\n\t"
        "ds_read_b64_tr_b16 v[92:93], v107 offset:53248\n\t"
        "v_mfma_f32_32x32x16_bf16 v[48:63], v[76:79], v[68:71], v[48:63]\n\t"
        "ds_read_b64_tr_b16 v[94:95], v107 offset:54272\n\t"
        "s_waitcnt lgkmcnt(3)\n\t"
        "v_mfma_f32_32x32x16_bf16 v[0:15], v[88:91], v[80:83], v[0:15]\n\t"
        "ds_read_b128 v[64:67], v102 offset:32768\n\t"
        "ds_read_b64_tr_b16 v[72:73], v106 offset:57344\n\t"
        "s_waitcnt lgkmcnt(4)\n\t"
        "v_mfma_f32_32x32x16_bf16 v[16:31], v[88:91], v[84:87], v[16:31]\n\t"
        "ds_read_b64_tr_b16 v[74:75], v106 offset:58368\n\t"
        "s_waitcnt lgkmcnt(3)\n\t"
        "v_mfma_f32_32x32x16_bf16 v[32:47], v[92:95], v[80:83], v[32:47]\n\t"
        "ds_read_b128 v[68:71], v102 offset:36864\n\t"
        "ds_read_b64_tr_b16 v[76:77], v107 offset:57344\n\t"
        "v_mfma_f32_32x32x16_bf16 v[48:63], v[92:95], v[84:87], v[48:63]\n\t"
        "ds_read_b64_tr_b16 v[78:79], v107 offset:58368\n\t"
        "s_waitcnt lgkmcnt(3)\n\t"
        "v_mfma_f32_32x32x16_bf16 v[0:15], v[72:75], v[64:67], v[0:15]\n\t"
        "ds_read_b128 v[80:83], v103 offset:32768\n\t"
        "ds_read_b64_tr_b16 v[88:89], v106 offset:61440\n\t"
        "s_waitcnt lgkmcnt(4)\n\t"
        "v_mfma_f32_32x32x16_bf16 v[16:31], v[72:75], v[68:71], v[16:31]\n\t"
        "ds_read_b64_tr_b16 v[90:91], v106 offset:62464\n\t"
        "ds_read_b32 v109, v108 offset:32\n\t"
        "s_waitcnt lgkmcnt(4)\n\t"
        "v_mfma_f32_32x32x16_bf16 v[32:47], v[76:79], v[64:67], v[32:47]\n\t"
        "ds_read_b128 v[84:87], v103 offset:36864\n\t"
        "ds_read_b64_tr_b16 v[92:93], v107 offset:61440\n\t"
        "v_mfma_f32_32x32x16_bf16 v[48:63], v[76:79], v[68:71], v[48:63]\n\t"
        "ds_read_b64_tr_b16 v[94:95], v107 offset:62464\n\t"
        "s_waitcnt lgkmcnt(3)\n\t"
        "v_readfirstlane_b32 %[t], v109\n\t"
        "s_nop 1\n\t"
        "s_cmp_ge_u32 %[t], 8\n\t"
        "s_cbranch_scc1 P1OK9_%=\n\t"
        "P1SP9_%=:\n\t"
        "ds_read_b32 v109, v108 offset:32\n\t"
        "s_waitcnt lgkmcnt(0)\n\t"
        "v_readfirstlane_b32 %[t], v109\n\t"
        "s_nop 1\n\t"
        "s_cmp_ge_u32 %[t], 8\n\t"
        "s_cbranch_scc0 P1SP9_%=\n\t"
        "P1OK9_%=:\n\t"
        "v_mfma_f32_32x32x16_bf16 v[0:15], v[88:91], v[80:83], v[0:15]\n\t"
        "ds_read_b128 v[64:67], v96 offset:0\n\t"
        "ds_read_b64_tr_b16 v[72:73], v104 offset:16384\n\t"
        "s_waitcnt lgkmcnt(4)\n\t"
        "v_mfma_f32_32x32x16_bf16 v[16:31], v[88:91], v[84:87], v[16:31]\n\t"
        "ds_read_b64_tr_b16 v[74:75], v104 offset:17408\n\t"
        "s_waitcnt lgkmcnt(3)\n\t"
        "v_mfma_f32_32x32x16_bf16 v[32:47], v[92:95], v[80:83], v[32:47]\n\t"
        "ds_read_b128 v[68:71], v96 offset:4096\n\t"
        "ds_read_b64_tr_b16 v[76:77], v105 offset:16384\n\t"
        "v_mfma_f32_32x32x16_bf16 v[48:63], v[92:95], v[84:87], v[48:63]\n\t"
        "ds_read_b64_tr_b16 v[78:79], v105 offset:17408\n\t"
        "s_waitcnt lgkmcnt(3)\n\t"
        "v_mfma_f32_32x32x16_bf16 v[0:15], v[72:75], v[64:67], v[0:15]\n\t"
        "ds_read_b128 v[80:83], v97 offset:0\n\t"
        "ds_read_b64_tr_b16 v[88:89], v104 offset:20480\n\t"
        "s_waitcnt lgkmcnt(4)\n\t"
        "v_mfma_f32_32x32x16_bf16 v[16:31], v[72:75], v[68:71], v[16:31]\n\t"
        "ds_read_b64_tr_b16 v[90:91], v104 offset:21504\n\t"
        "s_waitcnt lgkmcnt(3)\n\t"
        "v_mfma_f32_32x32x16_bf16 v[32:47], v[76:79], v[64:67], v[32:47]\n\t"
        "ds_read_b128 v[84:87], v97 offset:4096\n\t"
        "ds_read_b64_tr_b16 v[92:93], v105 offset:20480\n\t"
        "v_mfma_f32_32x32x16_bf16 v[48:63], v[76:79], v[68:71], v[48:63]\n\t"
        "ds_read_b64_tr_b16 v[94:95], v105 offset:21504\n\t"
        "s_waitcnt lgkmcnt(3)\n\t"
        "v_mfma_f32_32x32x16_bf16 v[0:15], v[88:91], v[80:83], v[0:15]\n\t"
        "ds_read_b128 v[64:67], v98 offset:0\n\t"
        "ds_read_b64_tr_b16 v[72:73], v104 offset:24576\n\t"
        "s_waitcnt lgkmcnt(4)\n\t"
        "v_mfma_f32_32x32x16_bf16 v[16:31], v[88:91], v[84:87], v[16:31]\n\t"
        "ds_read_b64_tr_b16 v[74:75], v104 offset:25600\n\t"
        "s_waitcnt lgkmcnt(3)\n\t"
        "v_mfma_f32_32x32x16_bf16 v[32:47], v[92:95], v[80:83], v[32:47]\n\t"
        "ds_read_b128 v[68:71], v98 offset:4096\n\t"
        "ds_read_b64_tr_b16 v[76:77], v105 offset:24576\n\t"
        "v_mfma_f32_32x32x16_bf16 v[48:63], v[92:95], v[84:87], v[48:63]\n\t"
        "ds_read_b64_tr_b16 v[78:79], v105 offset:25600\n\t"
        "s_waitcnt lgkmcnt(3)\n\t"
        "v_mfma_f32_32x32x16_bf16 v[0:15], v[72:75], v[64:67], v[0:15]\n\t"
        "ds_read_b128 v[80:83], v99 offset:0\n\t"
        "ds_read_b64_tr_b16 v[88:89], v104 offset:28672\n\t"
        "s_waitcnt lgkmcnt(4)\n\t"
        "v_mfma_f32_32x32x16_bf16 v[16:31], v[72:75], v[68:71], v[16:31]\n\t"
        "ds_read_b64_tr_b16 v[90:91], v104 offset:29696\n\t"
        "s_waitcnt lgkmcnt(3)\n\t"
        "v_mfma_f32_32x32x16_bf16 v[32:47], v[76:79], v[64:67], v[32:47]\n\t"
        "ds_read_b128 v[84:87], v99 offset:4096\n\t"
        "ds_read_b64_tr_b16 v[92:93], v105 offset:28672\n\t"
        "v_mfma_f32_32x32x16_bf16 v[48:63], v[76:79], v[68:71], v[48:63]\n\t"
        "ds_read_b64_tr_b16 v[94:95], v105 offset:29696\n\t"
        "s_waitcnt lgkmcnt(3)\n\t"
        "v_mfma_f32_32x32x16_bf16 v[0:15], v[88:91], v[80:83], v[0:15]\n\t"
        "s_waitcnt lgkmcnt(2)\n\t"
        "v_mfma_f32_32x32x16_bf16 v[16:31], v[88:91], v[84:87], v[16:31]\n\t"
        "s_waitcnt lgkmcnt(0)\n\t"
        "v_mfma_f32_32x32x16_bf16 v[32:47], v[92:95], v[80:83], v[32:47]\n\t"
        "v_mfma_f32_32x32x16_bf16 v[48:63], v[92:95], v[84:87], v[48:63]\n\t"
        "s_waitcnt lgkmcnt(0)\n\t"
        "s_mov_b64 exec, 1\n\t"
        "ds_add_u32 v108, v110 offset:68\n\t"
        "s_mov_b64 exec, -1\n\t"
        "s_nop 7\n\t"
        "s_nop 7\n\t"
        : "={v[0:15]}"(acc[0]), "={v[16:31]}"(acc[1]), "={v[32:47]}"(acc[2]), "={v[48:63]}"(acc[3]), [t] "=&s"(t)
        : "{v96}"(ax[0]), "{v97}"(ax[1]), "{v98}"(ax[2]), "{v99}"(ax[3]), "{v100}"(ax[4]), "{v101}"(ax[5]), "{v102}"(ax[6]), "{v103}"(ax[7]), "{v104}"(aw[0]), "{v105}"(aw[1]), "{v106}"(aw[2]), "{v107}"(aw[3]), "{v108}"(ac)
        : "v64", "v65", "v66", "v67", "v68", "v69", "v70", "v71", "v72", "v73", "v74", "v75", "v76", "v77", "v78", "v79", "v80", "v81", "v82", "v83", "v84", "v85", "v86", "v87", "v88", "v89", "v90", "v91", "v92", "v93", "v94", "v95", "v109", "v110", "memory", "scc");
}
__device__ __forceinline__ void p1_compute_v(f32x16 (&acc)[4], const unsigned (&ax)[8], const unsigned (&aw)[4], unsigned ac) {
    unsigned t;
    asm volatile(
        "v_mov_b32 v110, 1\n\t"
        "P1SP1_%=:\n\t"
        "ds_read_b32 v109, v108 offset:0\n\t"
        "s_waitcnt lgkmcnt(0)\n\t"
        "v_readfirstlane_b32 %[t], v109\n\t"
        "s_nop 1\n\t"
        "s_cmp_ge_u32 %[t], 8\n\t"
        "s_cbranch_scc0 P1SP1_%=\n\t"
        "P1OK1_%=:\n\t"
        "ds_read_b128 v[64:67], v96 offset:0\n\t"
        "ds_read_b64_tr_b16 v[72:73], v104 offset:16384\n\t"
        "ds_read_b64_tr_b16 v[74:75], v104 offset:17408\n\t"
        "ds_read_b128 v[68:71], v96 offset:4096\n\t"
        "ds_read_b64_tr_b16 v[76:77], v105 offset:16384\n\t"
        "ds_read_b64_tr_b16 v[78:79], v105 offset:17408\n\t"
        "s_waitcnt lgkmcnt(3)\n\t"
        "v_mfma_f32_32x32x16_bf16 v[0:15], v[64:67], v[72:75], 0\n\t"
        "ds_read_b128 v[80:83], v97 offset:0\n\t"
        "ds_read_b64_tr_b16 v[88:89], v104 offset:20480\n\t"
        "s_waitcnt lgkmcnt(4)\n\t"
        "v_mfma_f32_32x32x16_bf16 v[16:31], v[68:71], v[72:75], 0\n\t"
        "ds_read_b64_tr_b16 v[90:91], v104 offset:21504\n\t"
        "s_waitcnt lgkmcnt(3)\n\t"
        "v_mfma_f32_32x32x16_bf16 v[32:47], v[64:67], v[76:79], 0\n\t"
        "ds_read_b128 v[84:87], v97 offset:4096\n\t"
        "ds_read_b64_tr_b16 v[92:93], v105 offset:20480\n\t"
        "v_mfma_f32_32x32x16_bf16 v[48:63], v[68:71], v[76:79], 0\n\t"
        "ds_read_b64_tr_b16 v[94:95], v105 offset:21504\n\t"
        "s_waitcnt lgkmcnt(3)\n\t"
        "v_mfma_f32_32x32x16_bf16 v[0:15], v[80:83], v[88:91], v[0:15]\n\t"
        "ds_read_b128 v[64:67], v98 offset:0\n\t"
        "ds_read_b64_tr_b16 v[72:73], v104 offset:24576\n\t"
        "s_waitcnt lgkmcnt(4)\n\t"
        "v_mfma_f32_32x32x16_bf16 v[16:31], v[84:87], v[88:91], v[16:31]\n\t"
        "ds_read_b64_tr_b16 v[74:75], v104 offset:25600\n\t"
        "s_waitcnt lgkmcnt(3)\n\t"
        "v_mfma_f32_32x32x16_bf16 v[32:47], v[80:83], v[92:95], v[32:47]\n\t"
        "ds_read_b128 v[68:71], v98 offset:4096\n\t"
        "ds_read_b64_tr_b16 v[76:77], v105 offset:24576\n\t"
        "v_mfma_f32_32x32x16_bf16 v[48:63], v[84:87], v[92:95], v[48:63]\n\t"
        "ds_read_b64_tr_b16 v[78:79], v105 offset:25600\n\t"
        "s_waitcnt lgkmcnt(3)\n\t"
        "v_mfma_f32_32x32x16_bf16 v[0:15], v[64:67], v[72:75], v[0:15]\n\t"
        "ds_read_b128 v[80:83], v99 offset:0\n\t"
        "ds_read_b64_tr_b16 v[88:89], v104 offset:28672\n\t"
        "s_waitcnt lgkmcnt(4)\n\t"
        "v_mfma_f32_32x32x16_bf16 v[16:31], v[68:71], v[72:75], v[16:31]\n\t"
        "ds_read_b64_tr_b16 v[90:91], v104 offset:29696\n\t"
        "ds_read_b32 v109, v108 offset:4\n\t"
        "s_waitcnt lgkmcnt(4)\n\t"
        "v_mfma_f32_32x32x16_bf16 v[32:47], v[64:67], v[76:79], v[32:47]\n\t"
        "ds_read_b128 v[84:87], v99 offset:4096\n\t"
        "ds_read_b64_tr_b16 v[92:93], v105 offset:28672\n\t"
        "v_mfma_f32_32x32x16_bf16 v[48:63], v[68:71], v[76:79], v[48:63]\n\t"
        "ds_read_b64_tr_b16 v[94:95], v105 offset:29696\n\t"
        "s_waitcnt lgkmcnt(3)\n\t"
        "v_readfirstlane_b32 %[t], v109\n\t"
        "s_nop 1\n\t"
        "s_cmp_ge_u32 %[t], 8\n\t"
        "s_cbranch_scc1 P1OK2_%=\n\t"
        "P1SP2_%=:\n\t"
        "ds_read_b32 v109, v108 offset:4\n\t"
        "s_waitcnt lgkmcnt(0)\n\t"
        "v_readfirstlane_b32 %[t], v109\n\t"
        "s_nop 1\n\t"
        "s_cmp_ge_u32 %[t], 8\n\t"
        "s_cbranch_scc0 P1SP2_%=\n\t"
        "P1OK2_%=:\n\t"
        "v_mfma_f32_32x32x16_bf16 v[0:15], v[80:83], v[88:91], v[0:15]\n\t"
        "ds_read_b128 v[64:67], v96 offset:32768\n\t"
        "ds_read_b64_tr_b16 v[72:73], v104 offset:49152\n\t"
        "s_waitcnt lgkmcnt(4)\n\t"
        "v_mfma_f32_32x32x16_bf16 v[16:31], v[84:87], v[88:91], v[16:31]\n\t"
        "ds_read_b64_tr_b16 v[74:75], v104 offset:50176\n\t"
        "s_waitcnt lgkmcnt(3)\n\t"
        "s_mov_b64 exec, 1\n\t"
        "ds_add_u32 v108, v110 offset:36\n\t"
        "s_mov_b64 exec, -1\n\t"
        "v_mfma_f32_32x32x16_bf16 v[32:47], v[80:83], v[92:95], v[32:47]\n\t"
        "ds_read_b128 v[68:71], v96 offset:36864\n\t"
        "ds_read_b64_tr_b16 v[76:77], v105 offset:49152\n\t"
        "v_mfma_f32_32x32x16_bf16 v[48:63], v[84:87], v[92:95], v[48:63]\n\t"
        "ds_read_b64_tr_b16 v[78:79], v105 offset:50176\n\t"
        "s_waitcnt lgkmcnt(3)\n\t"
        "v_mfma_f32_32x32x16_bf16 v[0:15], v[64:67], v[72:75], v[0:15]\n\t"
        "ds_read_b128 v[80:83], v97 offset:32768\n\t"
        "ds_read_b64_tr_b16 v[88:89], v104 offset:53248\n\t"
        "s_waitcnt lgkmcnt(4)\n\t"
        "v_mfma_f32_32x32x16_bf16 v[16:31], v[68:71], v[72:75], v[16:31]\n\t"
        "ds_read_b64_tr_b16 v[90:91], v104 offset:54272\n\t"
        "s_waitcnt lgkmcnt(3)\n\t"
        "v_mfma_f32_32x32x16_bf16 v[32:47], v[64:67], v[76:79], v[32:47]\n\t"
        "ds_read_b128 v[84:87], v97 offset:36864\n\t"
        "ds_read_b64_tr_b16 v[92:93], v105 offset:53248\n\t"
        "v_mfma_f32_32x32x16_bf16 v[48:63], v[68:71], v[76:79], v[48:63]\n\t"
        "ds_read_b64_tr_b16 v[94:95], v105 offset:54272\n\t"
        "s_waitcnt lgkmcnt(3)\n\t"
        "v_mfma_f32_32x32x16_bf16 v[0:15], v[80:83], v[88:91], v[0:15]\n\t"
        "ds_read_b128 v[64:67], v98 offset:32768\n\t"
        "ds_read_b64_tr_b16 v[72:73], v104 offset:57344\n\t"
        "s_waitcnt lgkmcnt(4)\n\t"
        "v_mfma_f32_32x32x16_bf16 v[16:31], v[84:87], v[88:91], v[16:31]\n\t"
        "ds_read_b64_tr_b16 v[74:75], v104 offset:58368\n\t"
        "s_waitcnt lgkmcnt(3)\n\t"
        "v_mfma_f32_32x32x16_bf16 v[32:47], v[80:83], v[92:95], v[32:47]\n\t"
        "ds_read_b128 v[68:71], v98 offset:36864\n\t"
        "ds_read_b64_tr_b16 v[76:77], v105 offset:57344\n\t"
        "v_mfma_f32_32x32x16_bf16 v[48:63], v[84:87], v[92:95], v[48:63]\n\t"
        "ds_read_b64_tr_b16 v[78:79], v105 offset:58368\n\t"
        "s_waitcnt lgkmcnt(3)\n\t"
        "v_mfma_f32_32x32x16_bf16 v[0:15], v[64:67], v[72:75], v[0:15]\n\t"
        "ds_read_b128 v[80:83], v99 offset:32768\n\t"
        "ds_read_b64_tr_b16 v[88:89], v104 offset:61440\n\t"
        "s_waitcnt lgkmcnt(4)\n\t"
        "v_mfma_f32_32x32x16_bf16 v[16:31], v[68:71], v[72:75], v[16:31]\n\t"
        "ds_read_b64_tr_b16 v[90:91], v104 offset:62464\n\t"
        "ds_read_b32 v109, v108 offset:8\n\t"
        "s_waitcnt lgkmcnt(4)\n\t"
        "v_mfma_f32_32x32x16_bf16 v[32:47], v[64:67], v[76:79], v[32:47]\n\t"
        "ds_read_b128 v[84:87], v99 offset:36864\n\t"
        "ds_read_b64_tr_b16 v[92:93], v105 offset:61440\n\t"
        "v_mfma_f32_32x32x16_bf16 v[48:63], v[68:71], v[76:79], v[48:63]\n\t"
        "ds_read_b64_tr_b16 v[94:95], v105 offset:62464\n\t"
        "s_waitcnt lgkmcnt(3)\n\t"
        "v_readfirstlane_b32 %[t], v109\n\t"
        "s_nop 1\n\t"
        "s_cmp_ge_u32 %[t], 8\n\t"
        "s_cbranch_scc1 P1OK3_%=\n\t"
        "P1SP3_%=:\n\t"
        "ds_read_b32 v109, v108 offset:8\n\t"
        "s_waitcnt lgkmcnt(0)\n\t"
        "v_readfirstlane_b32 %[t], v109\n\t"
        "s_nop 1\n\t"
        "s_cmp_ge_u32 %[t], 8\n\t"
        "s_cbranch_scc0 P1SP3_%=\n\t"
        "P1OK3_%=:\n\t"
        "v_mfma_f32_32x32x16_bf16 v[0:15], v[80:83], v[88:91], v[0:15]\n\t"
        "ds_read_b128 v[64:67], v100 offset:0\n\t"
        "ds_read_b64_tr_b16 v[72:73], v106 offset:16384\n\t"
        "s_waitcnt lgkmcnt(4)\n\t"
        "v_mfma_f32_32x32x16_bf16 v[16:31], v[84:87], v[88:91], v[16:31]\n\t"
        "ds_read_b64_tr_b16 v[74:75], v106 offset:17408\n\t"
        "s_waitcnt lgkmcnt(3)\n\t"
        "s_mov_b64 exec, 1\n\t"
        "ds_add_u32 v108, v110 offset:40\n\t"
        "s_mov_b64 exec, -1\n\t"
        "v_mfma_f32_32x32x16_bf16 v[32:47], v[80:83], v[92:95], v[32:47]\n\t"
        "ds_read_b128 v[68:71], v100 offset:4096\n\t"
        "ds_read_b64_tr_b16 v[76:77], v107 offset:16384\n\t"
        "v_mfma_f32_32x32x16_bf16 v[48:63], v[84:87], v[92:95], v[48:63]\n\t"
        "ds_read_b64_tr_b16 v[78:79], v107 offset:17408\n\t"
        "s_waitcnt lgkmcnt(3)\n\t"
        "v_mfma_f32_32x32x16_bf16 v[0:15], v[64:67], v[72:75], v[0:15]\n\t"
        "ds_read_b128 v[80:83], v101 offset:0\n\t"
        "ds_read_b64_tr_b16 v[88:89], v106 offset:20480\n\t"
        "s_waitcnt lgkmcnt(4)\n\t"
        "v_mfma_f32_32x32x16_bf16 v[16:31], v[68:71], v[72:75], v[16:31]\n\t"
        "ds_read_b64_tr_b16 v[90:91], v106 offset:21504\n\t"
        "s_waitcnt lgkmcnt(3)\n\t"
        "v_mfma_f32_32x32x16_bf16 v[32:47], v[64:67], v[76:79], v[32:47]\n\t"
        "ds_read_b128 v[84:87], v101 offset:4096\n\t"
        "ds_read_b64_tr_b16 v[92:93], v107 offset:20480\n\t"
        "v_mfma_f32_32x32x16_bf16 v[48:63], v[68:71], v[76:79], v[48:63]\n\t"
        "ds_read_b64_tr_b16 v[94:95], v107 offset:21504\n\t"
        "s_waitcnt lgkmcnt(3)\n\t"
        "v_mfma_f32_32x32x16_bf16 v[0:15], v[80:83], v[88:91], v[0:15]\n\t"
        "ds_read_b128 v[64:67], v102 offset:0\n\t"
        "ds_read_b64_tr_b16 v[72:73], v106 offset:24576\n\t"
        "s_waitcnt lgkmcnt(4)\n\t"
        "v_mfma_f32_32x32x16_bf16 v[16:31], v[84:87], v[88:91], v[16:31]\n\t"
        "ds_read_b64_tr_b16 v[74:75], v106 offset:25600\n\t"
        "s_waitcnt lgkmcnt(3)\n\t"
        "v_mfma_f32_32x32x16_bf16 v[32:47], v[80:83], v[92:95], v[32:47]\n\t"
        "ds_read_b128 v[68:71], v102 offset:4096\n\t"
        "ds_read_b64_tr_b16 v[76:77], v107 offset:24576\n\t"
        "v_mfma_f32_32x32x16_bf16 v[48:63], v[84:87], v[92:95], v[48:63]\n\t"
        "ds_read_b64_tr_b16 v[78:79], v107 offset:25600\n\t"
        "s_waitcnt lgkmcnt(3)\n\t"
        "v_mfma_f32_32x32x16_bf16 v[0:15], v[64:67], v[72:75], v[0:15]\n\t"
        "ds_read_b128 v[80:83], v103 offset:0\n\t"
        "ds_read_b64_tr_b16 v[88:89], v106 offset:28672\n\t"
        "s_waitcnt lgkmcnt(4)\n\t"
        "v_mfma_f32_32x32x16_bf16 v[16:31], v[68:71], v[72:75], v[16:31]\n\t"
        "ds_read_b64_tr_b16 v[90:91], v106 offset:29696\n\t"
        "ds_read_b32 v109, v108 offset:12\n\t"
        "s_waitcnt lgkmcnt(4)\n\t"
        "v_mfma_f32_32x32x16_bf16 v[32:47], v[64:67], v[76:79], v[32:47]\n\t"
        "ds_read_b128 v[84:87], v103 offset:4096\n\t"
        "ds_read_b64_tr_b16 v[92:93], v107 offset:28672\n\t"
        "v_mfma_f32_32x32x16_bf16 v[48:63], v[68:71], v[76:79], v[48:63]\n\t"
        "ds_read_b64_tr_b16 v[94:95], v107 offset:29696\n\t"
        "s_waitcnt lgkmcnt(3)\n\t"
        "v_readfirstlane_b32 %[t], v109\n\t"
        "s_nop 1\n\t"
        "s_cmp_ge_u32 %[t], 8\n\t"
        "s_cbranch_scc1 P1OK4_%=\n\t"
        "P1SP4_%=:\n\t"
        "ds_read_b32 v109, v108 offset:12\n\t"
        "s_waitcnt lgkmcnt(0)\n\t"
        "v_readfirstlane_b32 %[t], v109\n\t"
        "s_nop 1\n\t"
        "s_cmp_ge_u32 %[t], 8\n\t"
        "s_cbranch_scc0 P1SP4_%=\n\t"
        "P1OK4_%=:\n\t"
        "v_mfma_f32_32x32x16_bf16 v[0:15], v[80:83], v[88:91], v[0:15]\n\t"
        "ds_read_b128 v[64:67], v100 offset:32768\n\t"
        "ds_read_b64_tr_b16 v[72:73], v106 offset:49152\n\t"
        "s_waitcnt lgkmcnt(4)\n\t"
        "v_mfma_f32_32x32x16_bf16 v[16:31], v[84:87], v[88:91], v[16:31]\n\t"
        "ds_read_b64_tr_b16 v[74:75], v106 offset:50176\n\t"
        "s_waitcnt lgkmcnt(3)\n\t"
        "s_mov_b64 exec, 1\n\t"
        "ds_add_u32 v108, v110 offset:44\n\t"
        "s_mov_b64 exec, -1\n\t"
        "v_mfma_f32_32x32x16_bf16 v[32:47], v[80:83], v[92:95], v[32:47]\n\t"
        "ds_read_b128 v[68:71], v100 offset:36864\n\t"
        "ds_read_b64_tr_b16 v[76:77], v107 offset:49152\n\t"
        "v_mfma_f32_32x32x16_bf16 v[48:63], v[84:87], v[92:95], v[48:63]\n\t"
        "ds_read_b64_tr_b16 v[78:79], v107 offset:50176\n\t"
        "s_waitcnt lgkmcnt(3)\n\t"
        "v_mfma_f32_32x32x16_bf16 v[0:15], v[64:67], v[72:75], v[0:15]\n\t"
        "ds_read_b128 v[80:83], v101 offset:32768\n\t"
        "ds_read_b64_tr_b16 v[88:89], v106 offset:53248\n\t"
        "s_waitcnt lgkmcnt(4)\n\t"
        "v_mfma_f32_32x32x16_bf16 v[16:31], v[68:71], v[72:75], v[16:31]\n\t"
        "ds_read_b64_tr_b16 v[90:91], v106 offset:54272\n\t"
        "s_waitcnt lgkmcnt(3)\n\t"
        "v_mfma_f32_32x32x16_bf16 v[32:47], v[64:67], v[76:79], v[32:47]\n\t"
        "ds_read_b128 v[84:87], v101 offset:36864\n\t"
        "ds_read_b64_tr_b16 v[92:93], v107 offset:53248\n\t"
        "v_mfma_f32_32x32x16_bf16 v[48:63], v[68:71], v[76:79], v[48:63]\n\t"
        "ds_read_b64_tr_b16 v[94:95], v107 offset:54272\n\t"
        "s_waitcnt lgkmcnt(3)\n\t"
        "v_mfma_f32_32x32x16_bf16 v[0:15], v[80:83], v[88:91], v[0:15]\n\t"
        "ds_read_b128 v[64:67], v102 offset:32768\n\t"
        "ds_read_b64_tr_b16 v[72:73], v106 offset:57344\n\t"
        "s_waitcnt lgkmcnt(4)\n\t"
        "v_mfma_f32_32x32x16_bf16 v[16:31], v[84:87], v[88:91], v[16:31]\n\t"
        "ds_read_b64_tr_b16 v[74:75], v106 offset:58368\n\t"
        "s_waitcnt lgkmcnt(3)\n\t"
        "v_mfma_f32_32x32x16_bf16 v[32:47], v[80:83], v[92:95], v[32:47]\n\t"
        "ds_read_b128 v[68:71], v102 offset:36864\n\t"
        "ds_read_b64_tr_b16 v[76:77], v107 offset:57344\n\t"
        "v_mfma_f32_32x32x16_bf16 v[48:63], v[84:87], v[92:95], v[48:63]\n\t"
        "ds_read_b64_tr_b16 v[78:79], v107 offset:58368\n\t"
        "s_waitcnt lgkmcnt(3)\n\t"
        "v_mfma_f32_32x32x16_bf16 v[0:15], v[64:67], v[72:75], v[0:15]\n\t"
        "ds_read_b128 v[80:83], v103 offset:32768\n\t"
        "ds_read_b64_tr_b16 v[88:89], v106 offset:61440\n\t"
        "s_waitcnt lgkmcnt(4)\n\t"
        "v_mfma_f32_32x32x16_bf16 v[16:31], v[68:71], v[72:75], v[16:31]\n\t"
        "ds_read_b64_tr_b16 v[90:91], v106 offset:62464\n\t"
        "ds_read_b32 v109, v108 offset:16\n\t"
        "s_waitcnt lgkmcnt(4)\n\t"
        "v_mfma_f32_32x32x16_bf16 v[32:47], v[64:67], v[76:79], v[32:47]\n\t"
        "ds_read_b128 v[84:87], v103 offset:36864\n\t"
        "ds_read_b64_tr_b16 v[92:93], v107 offset:61440\n\t"
        "v_mfma_f32_32x32x16_bf16 v[48:63], v[68:71], v[76:79], v[48:63]\n\t"
        "ds_read_b64_tr_b16 v[94:95], v107 offset:62464\n\t"
        "s_waitcnt lgkmcnt(3)\n\t"
        "v_readfirstlane_b32 %[t], v109\n\t"
        "s_nop 1\n\t"
        "s_cmp_ge_u32 %[t], 8\n\t"
        "s_cbranch_scc1 P1OK5_%=\n\t"
        "P1SP5_%=:\n\t"
        "ds_read_b32 v109, v108 offset:16\n\t"
        "s_waitcnt lgkmcnt(0)\n\t"
        "v_readfirstlane_b32 %[t], v109\n\t"
        "s_nop 1\n\t"
        "s_cmp_ge_u32 %[t], 8\n\t"
        "s_cbranch_scc0 P1SP5_%=\n\t"
        "P1OK5_%=:\n\t"
        "v_mfma_f32_32x32x16_bf16 v[0:15], v[80:83], v[88:91], v[0:15]\n\t"
        "ds_read_b128 v[64:67], v96 offset:0\n\t"
        "ds_read_b64_tr_b16 v[72:73], v104 offset:16384\n\t"
        "s_waitcnt lgkmcnt(4)\n\t"
        "v_mfma_f32_32x32x16_bf16 v[16:31], v[84:87], v[88:91], v[16:31]\n\t"
        "ds_read_b64_tr_b16 v[74:75], v104 offset:17408\n\t"
        "s_waitcnt lgkmcnt(3)\n\t"
        "s_mov_b64 exec, 1\n\t"
        "ds_add_u32 v108, v110 offset:48\n\t"
        "s_mov_b64 exec, -1\n\t"
        "v_mfma_f32_32x32x16_bf16 v[32:47], v[80:83], v[92:95], v[32:47]\n\t"
        "ds_read_b128 v[68:71], v96 offset:4096\n\t"
        "ds_read_b64_tr_b16 v[76:77], v105 offset:16384\n\t"
        "v_mfma_f32_32x32x16_bf16 v[48:63], v[84:87], v[92:95], v[48:63]\n\t"
        "ds_read_b64_tr_b16 v[78:79], v105 offset:17408\n\t"
        "s_waitcnt lgkmcnt(3)\n\t"
        "v_mfma_f32_32x32x16_bf16 v[0:15], v[64:67], v[72:75], v[0:15]\n\t"
        "ds_read_b128 v[80:83], v97 offset:0\n\t"
        "ds_read_b64_tr_b16 v[88:89], v104 offset:20480\n\t"
        "s_waitcnt lgkmcnt(4)\n\t"
        "v_mfma_f32_32x32x16_bf16 v[16:31], v[68:71], v[72:75], v[16:31]\n\t"
        "ds_read_b64_tr_b16 v[90:91], v104 offset:21504\n\t"
        "s_waitcnt lgkmcnt(3)\n\t"
        "v_mfma_f32_32x32x16_bf16 v[32:47], v[64:67], v[76:79], v[32:47]\n\t"
        "ds_read_b128 v[84:87], v97 offset:4096\n\t"
        "ds_read_b64_tr_b16 v[92:93], v105 offset:20480\n\t"
        "v_mfma_f32_32x32x16_bf16 v[48:63], v[68:71], v[76:79], v[48:63]\n\t"
        "ds_read_b64_tr_b16 v[94:95], v105 offset:21504\n\t"
        "s_waitcnt lgkmcnt(3)\n\t"
        "v_mfma_f32_32x32x16_bf16 v[0:15], v[80:83], v[88:91], v[0:15]\n\t"
        "ds_read_b128 v[64:67], v98 offset:0\n\t"
        "ds_read_b64_tr_b16 v[72:73], v104 offset:24576\n\t"
        "s_waitcnt lgkmcnt(4)\n\t"
        "v_mfma_f32_32x32x16_bf16 v[16:31], v[84:87], v[88:91], v[16:31]\n\t"
        "ds_read_b64_tr_b16 v[74:75], v104 offset:25600\n\t"
        "s_waitcnt lgkmcnt(3)\n\t"
        "v_mfma_f32_32x32x16_bf16 v[32:47], v[80:83], v[92:95], v[32:47]\n\t"
        "ds_read_b128 v[68:71], v98 offset:4096\n\t"
        "ds_read_b64_tr_b16 v[76:77], v105 offset:24576\n\t"
        "v_mfma_f32_32x32x16_bf16 v[48:63], v[84:87], v[92:95], v[48:63]\n\t"
        "ds_read_b64_tr_b16 v[78:79], v105 offset:25600\n\t"
        "s_waitcnt lgkmcnt(3)\n\t"
        "v_mfma_f32_32x32x16_bf16 v[0:15], v[64:67], v[72:75], v[0:15]\n\t"
        "ds_read_b128 v[80:83], v99 offset:0\n\t"
        "ds_read_b64_tr_b16 v[88:89], v104 offset:28672\n\t"
        "s_waitcnt lgkmcnt(4)\n\t"
        "v_mfma_f32_32x32x16_bf16 v[16:31], v[68:71], v[72:75], v[16:31]\n\t"
        "ds_read_b64_tr_b16 v[90:91], v104 offset:29696\n\t"
        "ds_read_b32 v109, v108 offset:20\n\t"
        "s_waitcnt lgkmcnt(4)\n\t"
        "v_mfma_f32_32x32x16_bf16 v[32:47], v[64:67], v[76:79], v[32:47]\n\t"
        "ds_read_b128 v[84:87], v99 offset:4096\n\t"
        "ds_read_b64_tr_b16 v[92:93], v105 offset:28672\n\t"
        "v_mfma_f32_32x32x16_bf16 v[48:63], v[68:71], v[76:79], v[48:63]\n\t"
        "ds_read_b64_tr_b16 v[94:95], v105 offset:29696\n\t"
        "s_waitcnt lgkmcnt(3)\n\t"
        "v_readfirstlane_b32 %[t], v109\n\t"
        "s_nop 1\n\t"
        "s_cmp_ge_u32 %[t], 8\n\t"
        "s_cbranch_scc1 P1OK6_%=\n\t"
        "P1SP6_%=:\n\t"
        "ds_read_b32 v109, v108 offset:20\n\t"
        "s_waitcnt lgkmcnt(0)\n\t"
        "v_readfirstlane_b32 %[t], v109\n\t"
        "s_nop 1\n\t"
        "s_cmp_ge_u32 %[t], 8\n\t"
        "s_cbranch_scc0 P1SP6_%=\n\t"
        "P1OK6_%=:\n\t"
        "v_mfma_f32_32x32x16_bf16 v[0:15], v[80:83], v[88:91], v[0:15]\n\t"
        "ds_read_b128 v[64:67], v96 offset:32768\n\t"
        "ds_read_b64_tr_b16 v[72:73], v104 offset:49152\n\t"
        "s_waitcnt lgkmcnt(4)\n\t"
        "v_mfma_f32_32x32x16_bf16 v[16:31], v[84:87], v[88:91], v[16:31]\n\t"
        "ds_read_b64_tr_b16 v[74:75], v104 offset:50176\n\t"
        "s_waitcnt lgkmcnt(3)\n\t"
        "s_mov_b64 exec, 1\n\t"
        "ds_add_u32 v108, v110 offset:52\n\t"
        "s_mov_b64 exec, -1\n\t"
        "v_mfma_f32_32x32x16_bf16 v[32:47], v[80:83], v[92:95], v[32:47]\n\t"
        "ds_read_b128 v[68:71], v96 offset:36864\n\t"
        "ds_read_b64_tr_b16 v[76:77], v105 offset:49152\n\t"
        "v_mfma_f32_32x32x16_bf16 v[48:63], v[84:87], v[92:95], v[48:63]\n\t"
        "ds_read_b64_tr_b16 v[78:79], v105 offset:50176\n\t"
        "s_waitcnt lgkmcnt(3)\n\t"
        "v_mfma_f32_32x32x16_bf16 v[0:15], v[64:67], v[72:75], v[0:15]\n\t"
        "ds_read_b128 v[80:83], v97 offset:32768\n\t"
        "ds_read_b64_tr_b16 v[88:89], v104 offset:53248\n\t"
        "s_waitcnt lgkmcnt(4)\n\t"
        "v_mfma_f32_32x32x16_bf16 v[16:31], v[68:71], v[72:75], v[16:31]\n\t"
        "ds_read_b64_tr_b16 v[90:91], v104 offset:54272\n\t"
        "s_waitcnt lgkmcnt(3)\n\t"
        "v_mfma_f32_32x32x16_bf16 v[32:47], v[64:67], v[76:79], v[32:47]\n\t"
        "ds_read_b128 v[84:87], v97 offset:36864\n\t"
        "ds_read_b64_tr_b16 v[92:93], v105 offset:53248\n\t"
        "v_mfma_f32_32x32x16_bf16 v[48:63], v[68:71], v[76:79], v[48:63]\n\t"
        "ds_read_b64_tr_b16 v[94:95], v105 offset:54272\n\t"
        "s_waitcnt lgkmcnt(3)\n\t"
        "v_mfma_f32_32x32x16_bf16 v[0:15], v[80:83], v[88:91], v[0:15]\n\t"
        "ds_read_b128 v[64:67], v98 offset:32768\n\t"
        "ds_read_b64_tr_b16 v[72:73], v104 offset:57344\n\t"
        "s_waitcnt lgkmcnt(4)\n\t"
        "v_mfma_f32_32x32x16_bf16 v[16:31], v[84:87], v[88:91], v[16:31]\n\t"
        "ds_read_b64_tr_b16 v[74:75], v104 offset:58368\n\t"
        "s_waitcnt lgkmcnt(3)\n\t"
        "v_mfma_f32_32x32x16_bf16 v[32:47], v[80:83], v[92:95], v[32:47]\n\t"
        "ds_read_b128 v[68:71], v98 offset:36864\n\t"
        "ds_read_b64_tr_b16 v[76:77], v105 offset:57344\n\t"
        "v_mfma_f32_32x32x16_bf16 v[48:63], v[84:87], v[92:95], v[48:63]\n\t"
        "ds_read_b64_tr_b16 v[78:79], v105 offset:58368\n\t"
        "s_waitcnt lgkmcnt(3)\n\t"
        "v_mfma_f32_32x32x16_bf16 v[0:15], v[64:67], v[72:75], v[0:15]\n\t"
        "ds_read_b128 v[80:83], v99 offset:32768\n\t"
        "ds_read_b64_tr_b16 v[88:89], v104 offset:61440\n\t"
        "s_waitcnt lgkmcnt(4)\n\t"
        "v_mfma_f32_32x32x16_bf16 v[16:31], v[68:71], v[72:75], v[16:31]\n\t"
        "ds_read_b64_tr_b16 v[90:91], v104 offset:62464\n\t"
        "ds_read_b32 v109, v108 offset:24\n\t"
        "s_waitcnt lgkmcnt(4)\n\t"
        "v_mfma_f32_32x32x16_bf16 v[32:47], v[64:67], v[76:79], v[32:47]\n\t"
        "ds_read_b128 v[84:87], v99 offset:36864\n\t"
        "ds_read_b64_tr_b16 v[92:93], v105 offset:61440\n\t"
        "v_mfma_f32_32x32x16_bf16 v[48:63], v[68:71], v[76:79], v[48:63]\n\t"
        "ds_read_b64_tr_b16 v[94:95], v105 offset:62464\n\t"
        "s_waitcnt lgkmcnt(3)\n\t"
        "v_readfirstlane_b32 %[t], v109\n\t"
        "s_nop 1\n\t"
        "s_cmp_ge_u32 %[t], 8\n\t"
        "s_cbranch_scc1 P1OK7_%=\n\t"
        "P1SP7_%=:\n\t"
        "ds_read_b32 v109, v108 offset:24\n\t"
        "s_waitcnt lgkmcnt(0)\n\t"
        "v_readfirstlane_b32 %[t], v109\n\t"
        "s_nop 1\n\t"
        "s_cmp_ge_u32 %[t], 8\n\t"
        "s_cbranch_scc0 P1SP7_%=\n\t"
        "P1OK7_%=:\n\t"
        "v_mfma_f32_32x32x16_bf16 v[0:15], v[80:83], v[88:91], v[0:15]\n\t"
        "ds_read_b128 v[64:67], v100 offset:0\n\t"
        "ds_read_b64_tr_b16 v[72:73], v106 offset:16384\n\t"
        "s_waitcnt lgkmcnt(4)\n\t"
        "v_mfma_f32_32x32x16_bf16 v[16:31], v[84:87], v[88:91], v[16:31]\n\t"
        "ds_read_b64_tr_b16 v[74:75], v106 offset:17408\n\t"
        "s_waitcnt lgkmcnt(3)\n\t"
        "v_mfma_f32_32x32x16_bf16 v[32:47], v[80:83], v[92:95], v[32:47]\n\t"
        "ds_read_b128 v[68:71], v100 offset:4096\n\t"
        "ds_read_b64_tr_b16 v[76:77], v107 offset:16384\n\t"
        "v_mfma_f32_32x32x16_bf16 v[48:63], v[84:87], v[92:95], v[48:63]\n\t"
        "ds_read_b64_tr_b16 v[78:79], v107 offset:17408\n\t"
        "s_waitcnt lgkmcnt(3)\n\t"
        "v_mfma_f32_32x32x16_bf16 v[0:15], v[64:67], v[72:75], v[0:15]\n\t"
        "ds_read_b128 v[80:83], v101 offset:0\n\t"
        "ds_read_b64_tr_b16 v[88:89], v106 offset:20480\n\t"
        "s_waitcnt lgkmcnt(4)\n\t"
        "v_mfma_f32_32x32x16_bf16 v[16:31], v[68:71], v[72:75], v[16:31]\n\t"
        "ds_read_b64_tr_b16 v[90:91], v106 offset:21504\n\t"
        "s_waitcnt lgkmcnt(3)\n\t"
        "v_mfma_f32_32x32x16_bf16 v[32:47], v[64:67], v[76:79], v[32:47]\n\t"
        "ds_read_b128 v[84:87], v101 offset:4096\n\t"
        "ds_read_b64_tr_b16 v[92:93], v107 offset:20480\n\t"
        "v_mfma_f32_32x32x16_bf16 v[48:63], v[68:71], v[76:79], v[48:63]\n\t"
        "ds_read_b64_tr_b16 v[94:95], v107 offset:21504\n\t"
        "s_waitcnt lgkmcnt(3)\n\t"
        "v_mfma_f32_32x32x16_bf16 v[0:15], v[80:83], v[88:91], v[0:15]\n\t"
        "ds_read_b128 v[64:67], v102 offset:0\n\t"
        "ds_read_b64_tr_b16 v[72:73], v106 offset:24576\n\t"
        "s_waitcnt lgkmcnt(4)\n\t"
        "v_mfma_f32_32x32x16_bf16 v[16:31], v[84:87], v[88:91], v[16:31]\n\t"
        "ds_read_b64_tr_b16 v[74:75], v106 offset:25600\n\t"
        "s_waitcnt lgkmcnt(3)\n\t"
        "v_mfma_f32_32x32x16_bf16 v[32:47], v[80:83], v[92:95], v[32:47]\n\t"
        "ds_read_b128 v[68:71], v102 offset:4096\n\t"
        "ds_read_b64_tr_b16 v[76:77], v107 offset:24576\n\t"
        "v_mfma_f32_32x32x16_bf16 v[48:63], v[84:87], v[92:95], v[48:63]\n\t"
        "ds_read_b64_tr_b16 v[78:79], v107 offset:25600\n\t"
        "s_waitcnt lgkmcnt(3)\n\t"
        "v_mfma_f32_32x32x16_bf16 v[0:15], v[64:67], v[72:75], v[0:15]\n\t"
        "ds_read_b128 v[80:83], v103 offset:0\n\t"
        "ds_read_b64_tr_b16 v[88:89], v106 offset:28672\n\t"
        "s_waitcnt lgkmcnt(4)\n\t"
        "v_mfma_f32_32x32x16_bf16 v[16:31], v[68:71], v[72:75], v[16:31]\n\t"
        "ds_read_b64_tr_b16 v[90:91], v106 offset:29696\n\t"
        "ds_read_b32 v109, v108 offset:28\n\t"
        "s_waitcnt lgkmcnt(4)\n\t"
        "v_mfma_f32_32x32x16_bf16 v[32:47], v[64:67], v[76:79], v[32:47]\n\t"
        "ds_read_b128 v[84:87], v103 offset:4096\n\t"
        "ds_read_b64_tr_b16 v[92:93], v107 offset:28672\n\t"
        "v_mfma_f32_32x32x16_bf16 v[48:63], v[68:71], v[76:79], v[48:63]\n\t"
        "ds_read_b64_tr_b16 v[94:95], v107 offset:29696\n\t"
        "s_waitcnt lgkmcnt(3)\n\t"
        "v_readfirstlane_b32 %[t], v109\n\t"
        "s_nop 1\n\t"
        "s_cmp_ge_u32 %[t], 8\n\t"
        "s_cbranch_scc1 P1OK8_%=\n\t"
        "P1SP8_%=:\n\t"
        "ds_read_b32 v109, v108 offset:28\n\t"
        "s_waitcnt lgkmcnt(0)\n\t"
        "v_readfirstlane_b32 %[t], v109\n\t"
        "s_nop 1\n\t"
        "s_cmp_ge_u32 %[t], 8\n\t"
        "s_cbranch_scc0 P1SP8_%=\n\t"
        "P1OK8_%=:\n\t"
        "v_mfma_f32_32x32x16_bf16 v[0:15], v[80:83], v[88:91], v[0:15]\n\t"
        "ds_read_b128 v[64:67], v100 offset:32768\n\t"
        "ds_read_b64_tr_b16 v[72:73], v106 offset:49152\n\t"
        "s_waitcnt lgkmcnt(4)\n\t"
        "v_mfma_f32_32x32x16_bf16 v[16:31], v[84:87], v[88:91], v[16:31]\n\t"
        "ds_read_b64_tr_b16 v[74:75], v106 offset:50176\n\t"
        "s_waitcnt lgkmcnt(3)\n\t"
        "v_mfma_f32_32x32x16_bf16 v[32:47], v[80:83], v[92:95], v[32:47]\n\t"
        "ds_read_b128 v[68:71], v100 offset:36864\n\t"
        "ds_read_b64_tr_b16 v[76:77], v107 offset:49152\n\t"
        "v_mfma_f32_32x32x16_bf16 v[48:63], v[84:87], v[92:95], v[48:63]\n\t"
        "ds_read_b64_tr_b16 v[78:79], v107 offset:50176\n\t"
        "s_waitcnt lgkmcnt(3)\n\t"
        "v_mfma_f32_32x32x16_bf16 v[0:15], v[64:67], v[72:75], v[0:15]\n\t"
        "ds_read_b128 v[80:83], v101 offset:32768\n\t"
        "ds_read_b64_tr_b16 v[88:89], v106 offset:53248\n\t"
        "s_waitcnt lgkmcnt(4)\n\t"
        "v_mfma_f32_32x32x16_bf16 v[16:31], v[68:71], v[72:75], v[16:31]\n\t"
        "ds_read_b64_tr_b16 v[90:91], v106 offset:54272\n\t"
        "s_waitcnt lgkmcnt(3)\n\t"
        "v_mfma_f32_32x32x16_bf16 v[32:47], v[64:67], v[76:79], v[32:47]\n\t"
        "ds_read_b128 v[84:87], v101 offset:36864\n\t"
        "ds_read_b64_tr_b16 v[92:93], v107 offset:53248\n\t"
        "v_mfma_f32_32x32x16_bf16 v[48:63], v[68:71], v[76:79], v[48:63]\n\t"
        "ds_read_b64_tr_b16 v[94:95], v107 offset:54272\n\t"
        "s_waitcnt lgkmcnt(3)\n\t"
        "v_mfma_f32_32x32x16_bf16 v[0:15], v[80:83], v[88:91], v[0:15]\n\t"
        "ds_read_b128 v[64:67], v102 offset:32768\n\t"
        "ds_read_b64_tr_b16 v[72:73], v106 offset:57344\n\t"
        "s_waitcnt lgkmcnt(4)\n\t"
        "v_mfma_f32_32x32x16_bf16 v[16:31], v[84:87], v[88:91], v[16:31]\n\t"
        "ds_read_b64_tr_b16 v[74:75], v106 offset:58368\n\t"
        "s_waitcnt lgkmcnt(3)\n\t"
        "v_mfma_f32_32x32x16_bf16 v[32:47], v[80:83], v[92:95], v[32:47]\n\t"
        "ds_read_b128 v[68:71], v102 offset:36864\n\t"
        "ds_read_b64_tr_b16 v[76:77], v107 offset:57344\n\t"
        "v_mfma_f32_32x32x16_bf16 v[48:63], v[84:87], v[92:95], v[48:63]\n\t"
        "ds_read_b64_tr_b16 v[78:79], v107 offset:58368\n\t"
        "s_waitcnt lgkmcnt(3)\n\t"
        "v_mfma_f32_32x32x16_bf16 v[0:15], v[64:67], v[72:75], v[0:15]\n\t"
        "ds_read_b128 v[80:83], v103 offset:32768\n\t"
        "ds_read_b64_tr_b16 v[88:89], v106 offset:61440\n\t"
        "s_waitcnt lgkmcnt(4)\n\t"
        "v_mfma_f32_32x32x16_bf16 v[16:31], v[68:71], v[72:75], v[16:31]\n\t"
        "ds_read_b64_tr_b16 v[90:91], v106 offset:62464\n\t"
        "ds_read_b32 v109, v108 offset:32\n\t"
        "s_waitcnt lgkmcnt(4)\n\t"
        "v_mfma_f32_32x32x16_bf16 v[32:47], v[64:67], v[76:79], v[32:47]\n\t"
        "ds_read_b128 v[84:87], v103 offset:36864\n\t"
        "ds_read_b64_tr_b16 v[92:93], v107 offset:61440\n\t"
        "v_mfma_f32_32x32x16_bf16 v[48:63], v[68:71], v[76:79], v[48:63]\n\t"
        "ds_read_b64_tr_b16 v[94:95], v107 offset:62464\n\t"
        "s_waitcnt lgkmcnt(3)\n\t"
        "v_readfirstlane_b32 %[t], v109\n\t"
        "s_nop 1\n\t"
        "s_cmp_ge_u32 %[t], 8\n\t"
        "s_cbranch_scc1 P1OK9_%=\n\t"
        "P1SP9_%=:\n\t"
        "ds_read_b32 v109, v108 offset:32\n\t"
        "s_waitcnt lgkmcnt(0)\n\t"
        "v_readfirstlane_b32 %[t], v109\n\t"
        "s_nop 1\n\t"
        "s_cmp_ge_u32 %[t], 8\n\t"
        "s_cbranch_scc0 P1SP9_%=\n\t"
        "P1OK9_%=:\n\t"
        "v_mfma_f32_32x32x16_bf16 v[0:15], v[80:83], v[88:91], v[0:15]\n\t"
        "ds_read_b128 v[64:67], v96 offset:0\n\t"
        "ds_read_b64_tr_b16 v[72:73], v104 offset:16384\n\t"
        "s_waitcnt lgkmcnt(4)\n\t"
        "v_mfma_f32_32x32x16_bf16 v[16:31], v[84:87], v[88:91], v[16:31]\n\t"
        "ds_read_b64_tr_b16 v[74:75], v104 offset:17408\n\t"
        "s_waitcnt lgkmcnt(3)\n\t"
        "v_mfma_f32_32x32x16_bf16 v[32:47], v[80:83], v[92:95], v[32:47]\n\t"
        "ds_read_b128 v[68:71], v96 offset:4096\n\t"
        "ds_read_b64_tr_b16 v[76:77], v105 offset:16384\n\t"
        "v_mfma_f32_32x32x16_bf16 v[48:63], v[84:87], v[92:95], v[48:63]\n\t"
        "ds_read_b64_tr_b16 v[78:79], v105 offset:17408\n\t"
        "s_waitcnt lgkmcnt(3)\n\t"
        "v_mfma_f32_32x32x16_bf16 v[0:15], v[64:67], v[72:75], v[0:15]\n\t"
        "ds_read_b128 v[80:83], v97 offset:0\n\t"
        "ds_read_b64_tr_b16 v[88:89], v104 offset:20480\n\t"
        "s_waitcnt lgkmcnt(4)\n\t"
        "v_mfma_f32_32x32x16_bf16 v[16:31], v[68:71], v[72:75], v[16:31]\n\t"
        "ds_read_b64_tr_b16 v[90:91], v104 offset:21504\n\t"
        "s_waitcnt lgkmcnt(3)\n\t"
        "v_mfma_f32_32x32x16_bf16 v[32:47], v[64:67], v[76:79], v[32:47]\n\t"
        "ds_read_b128 v[84:87], v97 offset:4096\n\t"
        "ds_read_b64_tr_b16 v[92:93], v105 offset:20480\n\t"
        "v_mfma_f32_32x32x16_bf16 v[48:63], v[68:71], v[76:79], v[48:63]\n\t"
        "ds_read_b64_tr_b16 v[94:95], v105 offset:21504\n\t"
        "s_waitcnt lgkmcnt(3)\n\t"
        "v_mfma_f32_32x32x16_bf16 v[0:15], v[80:83], v[88:91], v[0:15]\n\t"
        "ds_read_b128 v[64:67], v98 offset:0\n\t"
        "ds_read_b64_tr_b16 v[72:73], v104 offset:24576\n\t"
        "s_waitcnt lgkmcnt(4)\n\t"
        "v_mfma_f32_32x32x16_bf16 v[16:31], v[84:87], v[88:91], v[16:31]\n\t"
        "ds_read_b64_tr_b16 v[74:75], v104 offset:25600\n\t"
        "s_waitcnt lgkmcnt(3)\n\t"
        "v_mfma_f32_32x32x16_bf16 v[32:47], v[80:83], v[92:95], v[32:47]\n\t"
        "ds_read_b128 v[68:71], v98 offset:4096\n\t"
        "ds_read_b64_tr_b16 v[76:77], v105 offset:24576\n\t"
        "v_mfma_f32_32x32x16_bf16 v[48:63], v[84:87], v[92:95], v[48:63]\n\t"
        "ds_read_b64_tr_b16 v[78:79], v105 offset:25600\n\t"
        "s_waitcnt lgkmcnt(3)\n\t"
        "v_mfma_f32_32x32x16_bf16 v[0:15], v[64:67], v[72:75], v[0:15]\n\t"
        "ds_read_b128 v[80:83], v99 offset:0\n\t"
        "ds_read_b64_tr_b16 v[88:89], v104 offset:28672\n\t"
        "s_waitcnt lgkmcnt(4)\n\t"
        "v_mfma_f32_32x32x16_bf16 v[16:31], v[68:71], v[72:75], v[16:31]\n\t"
        "ds_read_b64_tr_b16 v[90:91], v104 offset:29696\n\t"
        "s_waitcnt lgkmcnt(3)\n\t"
        "v_mfma_f32_32x32x16_bf16 v[32:47], v[64:67], v[76:79], v[32:47]\n\t"
        "ds_read_b128 v[84:87], v99 offset:4096\n\t"
        "ds_read_b64_tr_b16 v[92:93], v105 offset:28672\n\t"
        "v_mfma_f32_32x32x16_bf16 v[48:63], v[68:71], v[76:79], v[48:63]\n\t"
        "ds_read_b64_tr_b16 v[94:95], v105 offset:29696\n\t"
        "s_waitcnt lgkmcnt(3)\n\t"
        "v_mfma_f32_32x32x16_bf16 v[0:15], v[80:83], v[88:91], v[0:15]\n\t"
        "s_waitcnt lgkmcnt(2)\n\t"
        "v_mfma_f32_32x32x16_bf16 v[16:31], v[84:87], v[88:91], v[16:31]\n\t"
        "s_waitcnt lgkmcnt(0)\n\t"
        "v_mfma_f32_32x32x16_bf16 v[32:47], v[80:83], v[92:95], v[32:47]\n\t"
        "v_mfma_f32_32x32x16_bf16 v[48:63], v[84:87], v[92:95], v[48:63]\n\t"
        "s_waitcnt lgkmcnt(0)\n\t"
        "s_mov_b64 exec, 1\n\t"
        "ds_add_u32 v108, v110 offset:68\n\t"
        "s_mov_b64 exec, -1\n\t"
        "s_nop 7\n\t"
        "s_nop 7\n\t"
        : "={v[0:15]}"(acc[0]), "={v[16:31]}"(acc[1]), "={v[32:47]}"(acc[2]), "={v[48:63]}"(acc[3]), [t] "=&s"(t)
        : "{v96}"(ax[0]), "{v97}"(ax[1]), "{v98}"(ax[2]), "{v99}"(ax[3]), "{v100}"(ax[4]), "{v101}"(ax[5]), "{v102}"(ax[6]), "{v103}"(ax[7]), "{v104}"(aw[0]), "{v105}"(aw[1]), "{v106}"(aw[2]), "{v107}"(aw[3]), "{v108}"(ac)
        : "v64", "v65", "v66", "v67", "v68", "v69", "v70", "v71", "v72", "v73", "v74", "v75", "v76", "v77", "v78", "v79", "v80", "v81", "v82", "v83", "v84", "v85", "v86", "v87", "v88", "v89", "v90", "v91", "v92", "v93", "v94", "v95", "v109", "v110", "memory", "scc");
}

#define WAITV_BAR(N) asm volatile("s_waitcnt vmcnt(" #N ")\n\ts_barrier" ::: "memory")
__device__ __forceinline__ void p1_loader(int w, int l, int b, const u16* __restrict__ x, const u16* __restrict__ wqkv, unsigned lds0 STARG) {
    const int xcd = b & 7, idx = b >> 3;
    const int rb = (xcd >> 1) * 4 + (idx & 3), cb = (xcd & 1) * 7 + (idx >> 2);
    const int lane = lane_id();
    const char *xg, *wg;
    {
        const int row = 8 * l + (lane >> 3);
        xg = (const char*)x + (size_t)(rb * 128 + row) * 1152 + (((lane & 7) ^ ((row >> 1) & 7)) * 16);
        const int kr = 4 * l + (lane >> 4);
        int c = (lane & 15) ^ (4 * (kr & 3));
        if (cb == 13) c &= 7;
        wg = (const char*)wqkv + (size_t)kr * 3456 + cb * 256 + c * 16;
    }
    auto issue = [&](int st) {
        const unsigned dst = lds0 + L_RING + (st & 3) * 32768 + l * 1024;
        glds16(xg + st * 128, rfl(dst));
        glds16(wg + (size_t)(st * 64) * 3456, rfl(dst + 16384));
        glds16(xg + st * 128 + 64 * 1152, rfl(dst + 8192));
        glds16(wg + (size_t)(st * 64 + 32) * 3456, rfl(dst + 24576));
    };
    const unsigned ring = lds0 + L_SYNC + SY_RING * 4;
    auto landed = [&](int st) { if (lane == 0) lds_add(ring + st * 4); };
    issue(0); issue(1); issue(2); issue(3);
    ST(22);
    asm volatile("s_waitcnt vmcnt(12)" ::: "memory"); landed(0); ST(24);
    asm volatile("s_waitcnt vmcnt(8)" ::: "memory"); landed(1);
#pragma unroll
    for (int s = 0; s < 7; ++s) {
        if (s + 4 < 9) lds_spin(ring + (9 + s) * 4, 4);
        if (s < 6) asm volatile("s_waitcnt vmcnt(4)" ::: "memory"); else asm volatile("s_waitcnt vmcnt(0)" ::: "memory");
        landed(s + 2);
        if (s == 0) { ST(26); STC(31); } if (s == 2) ST(27); if (s == 4) ST(28); if (s == 6) { ST(29); STC(32); }
        if (s + 4 < 9) issue(s + 4);
    }
}

__device__ __forceinline__ void p1_gemm(int b, int w, const u16* __restrict__ x, const u16* __restrict__ wqkv, char* __restrict__ ws, rsrc_t R, unsigned lds0 STARG) {
    const int xcd = b & 7, idx = b >> 3;
    const int rb = (xcd >> 1) * 4 + (idx & 3), cb = (xcd & 1) * 7 + (idx >> 2);
    const int lane = lane_id(), r = lane & 31, hi = lane >> 5;
    const int cp = w & 1, th = w >> 1;
    const bool vt = cb >= 9;
    const unsigned long long tabA = vt ? 0ull : *(const unsigned long long*)(P1_TAB + cb * 32 + cp * 16), tabB = vt ? 0ull : *(const unsigned long long*)(P1_TAB + cb * 32 + cp * 16 + 8);
    const unsigned kind4 = vt ? 0u : *(const unsigned*)(P1_KIND + cb * 8 + cp * 4);
    f32x16 acc[4];
    {
        unsigned ax[8], aw[4];
        const unsigned xrd = L_RING + th * 8192 + r * 128, sw = hi ^ ((r >> 1) & 7);
#pragma unroll
        for (int kk = 0; kk < 4; ++kk) { ax[kk] = lds0 + xrd + ((sw ^ (2 * kk)) * 16); ax[4 + kk] = ax[kk] + 65536; }
        const int q = (lane & 15) >> 2, p = lane & 3, blk = (lane >> 4) & 1;
#pragma unroll
        for (int ci = 0; ci < 2; ++ci) {
            const int g = vt ? 8 * (2 * cp + ci) + 4 * blk + p : (int)(((ci ? tabB : tabA) >> (8 * (4 * blk + p))) & 0xff);
            aw[ci] = lds0 + L_RING + (8 * hi + q) * 256 + (((g >> 1) ^ (4 * q)) * 16) + (g & 1) * 8; aw[2 + ci] = aw[ci] + 65536;
        }
        const unsigned ac = lds0 + L_SYNC + SY_RING * 4;
        if (vt) p1_compute_v(acc, ax, aw, ac); else p1_compute_qk(acc, ax, aw, ac);
    }
    ST(3);
    const int tile0 = rb * 4 + 2 * th;
    const unsigned kvt = WS_KV + (tile0 >> 2) * KVG;
#pragma unroll
    for (int ci = 0; ci < 2; ++ci) {
        const int ct = 2 * cp + ci;
        const unsigned long long tab8 = ci ? tabB : tabA;
        if (!vt) {
#pragma unroll
            for (int j = 0; j < 2; ++j) {
                if ((kind4 >> (8 * (2 * ci + j))) & 1) {
                    const int G = cb * 32 + (int)((tab8 >> (32 * j)) & 0xff);
                    const bool isk = G >= 144;
                    const int Gs = isk ? G - 144 : G, hp = Gs / 9, h = 2 * hp + ((Gs - 9 * hp) > 4 ? 1 : 0);
                    const float sc = isk ? 1.0f : QSCALE;
                    const unsigned d = (isk ? kvt + h * 16 * KVG + (tile0 & 3) * 1024 : WS_QF0 + (h * 64 + tile0) * 1024) + lane * 16;
                    float na = 0.f, nb = 0.f;
#pragma unroll
                    for (int i = 0; i < 2; ++i) {
                        const f32x16& a = acc[2 * ci + i];
                        const u32x4 o = {cvtpk(a[8 * j] * sc, a[8 * j + 1] * sc), cvtpk(a[8 * j + 2] * sc, a[8 * j + 3] * sc), cvtpk(a[8 * j + 4] * sc, a[8 * j + 5] * sc), cvtpk(a[8 * j + 6] * sc, a[8 * j + 7] * sc)};
                        st128(R, d + i * 1024, o);
                        float e0 = bflo(o[0]), e1 = bfhi(o[0]), e2 = bflo(o[1]), e3 = bfhi(o[1]); na = fmaxf(na, e0 * e0 + e1 * e1 + e2 * e2 + e3 * e3);
                        e0 = bflo(o[2]); e1 = bfhi(o[2]); e2 = bflo(o[3]); e3 = bfhi(o[3]); nb = fmaxf(nb, e0 * e0 + e1 * e1 + e2 * e2 + e3 * e3);
                    }
                    if (isk) {
                        na = half_max_nn(na); nb = half_max_nn(nb);
                        if (r == 31) { st32(R, WS_KMX + ((h * 5 + hi) * 32 + (tile0 >> 1)) * 4, __float_as_uint(na)); st32(R, WS_KMX + ((h * 5 + 2 + hi) * 32 + (tile0 >> 1)) * 4, __float_as_uint(nb)); }
                    }
                } else {
#pragma unroll
                    for (int mm = 0; mm < 2; ++mm) {
                        const int m = 2 * j + mm;
                        const int G = cb * 32 + (int)((tab8 >> (8 * (2 * m + hi))) & 0xff);
                        const bool isk = G >= 144;
                        const int Gs = isk ? G - 144 : G, hp = Gs / 9, pos = Gs - 9 * hp;
                        const bool rem = pos == 4;
                        const int h = 2 * hp + (pos > 4 ? 1 : 0), g = pos > 4 ? pos - 5 : pos;
                        const float sc = isk ? 1.0f : QSCALE;
                        unsigned d, tstr, hstr;
                        if (!rem) { d = (isk ? kvt + h * 16 * KVG + (tile0 & 3) * 1024 : WS_QF0 + (h * 64 + tile0) * 1024) + (r + 32 * (g & 1)) * 16 + (g >> 1) * 8; tstr = 1024; hstr = 0; }
                        else { d = isk ? kvt + h * 16 * KVG + 4096 + (tile0 & 3) * 256 + r * 8 : WS_QF1 + (h * SEQ + tile0 * 32 + r) * 8; tstr = 256; hstr = isk ? 16 * KVG : SEQ * 8; }
                        float ma = 0.f, mb = 0.f;
#pragma unroll
                        for (int i = 0; i < 2; ++i) {
                            const f32x16& a = acc[2 * ci + i];
                            const unsigned p0 = cvtpk(a[4 * m] * sc, a[4 * m + 1] * sc), p1 = cvtpk(a[4 * m + 2] * sc, a[4 * m + 3] * sc);
                            const unsigned one = isk ? 0x00003f80u : 0u;
                            st64(R, d + i * tstr, u32x2{p0, rem ? one : p1});
                            if (rem) st64(R, d + i * tstr + hstr, u32x2{p1, one});
                            const float n0 = bflo(p0) * bflo(p0) + bfhi(p0) * bfhi(p0), n1 = bflo(p1) * bflo(p1) + bfhi(p1) * bfhi(p1);
                            ma = fmaxf(ma, rem ? n0 : n0 + n1); mb = fmaxf(mb, n1);
                        }
                        ma = half_max_nn(ma); mb = half_max_nn(mb);
                        if (isk && r == 31) {
                            st32(R, WS_KMX + ((h * 5 + (rem ? 4 : g)) * 32 + (tile0 >> 1)) * 4, __float_as_uint(ma));
                            if (rem) st32(R, WS_KMX + (((h + 1) * 5 + 4) * 32 + (tile0 >> 1)) * 4, __float_as_uint(mb));
                        }
                    }
                }
            }
        } else if (cb * 128 + ct * 32 < 1728) {
            const int c = cb * 128 + ct * 32 + r - 1152, h = c / 18, d = c - 18 * h;
            const int row = (h & 1) ? (d >= 2 ? d - 2 : 16 + d) : d;
            const unsigned vb = kvt + h * 16 * KVG + 5120 + (tile0 & 3) * 2 * 640 + hi * 320;
#pragma unroll
            for (int i = 0; i < 2; ++i)
#pragma unroll
                for (int s = 0; s < 2; ++s) {
                    const f32x16& a = acc[2 * ci + i];
                    const unsigned pz = vb + (2 * i + s) * 640;
                    st128(R, pz + row * 16, u32x4{cvtpk(a[8 * s], a[8 * s + 1]), cvtpk(a[8 * s + 2], a[8 * s + 3]), cvtpk(a[8 * s + 4], a[8 * s + 5]), cvtpk(a[8 * s + 6], a[8 * s + 7])});
                    if (d < 2) { const unsigned f = d ? 0u : 0x3f803f80u; st128(R, pz + (18 + d) * 16, u32x4{f, f, f, f}); }
                }
        }
    }
    ST(4);
    publish(ws, FL_P1 + (cb * 16 + rb) * 4, lds0 + L_SYNC + SY_P1 * 4, 4, get_epoch(lds0));
    ST(5);
}

template <int NW>
__device__ __forceinline__ void c1_kv(int w, int task, int t, const u16* __restrict__ bk, const u16* __restrict__ bv, const u16* __restrict__ shiftk, char* __restrict__ ws, rsrc_t R,
                                      unsigned sc, unsigned cnt, unsigned bar, unsigned lds0 STARG) {
    constexpr int NI = 8 / NW;
    const int tb = task / 9, cg = task - 9 * tb, t0 = tb * 64, c0 = cg * 64;
    const int cc = t & 7, ch = c0 + cc * 8;
    u32x4 kr[NI][4], vr[NI], sw[4];
#pragma unroll
    for (int n = 0; n < NI; ++n) {
        const int tk = (t >> 3) + n * NW * 8, tt0 = t0 + tk;
#pragma unroll
        for (int j = 0; j < 4; ++j) { const int tt = tt0 - 3 + j; kr[n][j] = *(const u32x4*)(bk + (size_t)(tt >= 0 ? tt : 0) * DM + ch); }
        vr[n] = *(const u32x4*)(bv + (size_t)tt0 * DM + ch);
    }
#pragma unroll
    for (int j = 0; j < 4; ++j) sw[j] = *(const u32x4*)(shiftk + (size_t)ch * 4 + j * 8);
#pragma unroll
    for (int n = 0; n < NI; ++n) {
        const int tk = (t >> 3) + n * NW * 8;
#pragma unroll
        for (int j = 0; j < 3; ++j) if (t0 + tk - 3 + j < 0) kr[n][j] = u32x4{0, 0, 0, 0};
#pragma unroll
        for (int e2 = 0; e2 < 4; ++e2) {
            const u32x4 tw = sw[e2];
            float s0 = bflo(kr[n][0][e2]) * bfhi(tw[1]), s1 = bfhi(kr[n][0][e2]) * bfhi(tw[3]);
            s0 = fmaf(bflo(kr[n][1][e2]), bflo(tw[1]), s0); s1 = fmaf(bfhi(kr[n][1][e2]), bflo(tw[3]), s1);
            s0 = fmaf(bflo(kr[n][2][e2]), bfhi(tw[0]), s0); s1 = fmaf(bfhi(kr[n][2][e2]), bfhi(tw[2]), s1);
            s0 = fmaf(bflo(kr[n][3][e2]), bflo(tw[0]), s0); s1 = fmaf(bfhi(kr[n][3][e2]), bflo(tw[2]), s1);
            const unsigned pr = cvtpk(s0 * bflo(vr[n][e2]), s1 * bfhi(vr[n][e2]));
            ldswr<u16>(sc + (cc * 8 + 2 * e2) * 144 + (63 - tk) * 2, (u16)pr);
            ldswr<u16>(sc + (cc * 8 + 2 * e2 + 1) * 144 + (63 - tk) * 2, (u16)(pr >> 16));
        }
    }
    sw_barrier(bar, NW);
#pragma unroll
    for (int n = 0; n < NI; ++n) {
        const int chl = (t >> 3) + n * NW * 8, c = t & 7;
        const unsigned xo = WS_XP + (c0 + chl) * (XPS * 2);
        st128(R, xo + (2016 - t0 + 8 * c) * 2, ldsrd<u32x4>(sc + chl * 144 + c * 16));
        if (tb == 31 && c < 4) st128(R, xo + 16 * c, u32x4{0, 0, 0, 0});
        if (tb == 0) st128(R, xo + (2080 + 8 * c) * 2, u32x4{0, 0, 0, 0});
    }
    publish(ws, FL_C1 + (cg * 32 + tb) * 4, cnt, NW, get_epoch(lds0));
}

__device__ __forceinline__ void conv_body(f32x16& y0, f32x16& y1, unsigned aA, unsigned aB) {
    asm volatile(
        "ds_read_b64 v[36:37], v0 offset:2048\n\t"
        "ds_read_b64 v[38:39], v0 offset:2056\n\t"
        "ds_read_b128 v[60:63], v1 offset:2560\n\t"
        "ds_read_b128 v[52:55], v1 offset:0\n\t"
        "ds_read_b64 v[44:45], v0 offset:0\n\t"
        "ds_read_b64 v[46:47], v0 offset:8\n\t"
        "v_mov_b32 v4, 0\n\t"
        "v_mov_b32 v5, 0\n\t"
        "v_mov_b32 v6, 0\n\t"
        "v_mov_b32 v7, 0\n\t"
        "v_mov_b32 v8, 0\n\t"
        "v_mov_b32 v9, 0\n\t"
        "v_mov_b32 v10, 0\n\t"
        "v_mov_b32 v11, 0\n\t"
        "v_mov_b32 v12, 0\n\t"
        "v_mov_b32 v13, 0\n\t"
        "v_mov_b32 v14, 0\n\t"
        "v_mov_b32 v15, 0\n\t"
        "v_mov_b32 v16, 0\n\t"
        "v_mov_b32 v17, 0\n\t"
        "v_mov_b32 v18, 0\n\t"
        "v_mov_b32 v19, 0\n\t"
        "v_mov_b32 v20, 0\n\t"
        "v_mov_b32 v21, 0\n\t"
        "v_mov_b32 v22, 0\n\t"
        "v_mov_b32 v23, 0\n\t"
        "v_mov_b32 v24, 0\n\t"
        "v_mov_b32 v25, 0\n\t"
        "v_mov_b32 v26, 0\n\t"
        "v_mov_b32 v27, 0\n\t"
        "v_mov_b32 v28, 0\n\t"
        "v_mov_b32 v29, 0\n\t"
        "v_mov_b32 v30, 0\n\t"
        "v_mov_b32 v31, 0\n\t"
        "v_mov_b32 v32, 0\n\t"
        "v_mov_b32 v33, 0\n\t"
        "v_mov_b32 v34, 0\n\t"
        "v_mov_b32 v35, 0\n\t"
        "s_waitcnt lgkmcnt(0)\n\t"
        "ds_read_b64 v[40:41], v0 offset:2080\n\t"
        "ds_read_b64 v[42:43], v0 offset:2088\n\t"
        "ds_read_b128 v[64:67], v1 offset:2592\n\t"
        "ds_read_b128 v[56:59], v1 offset:32\n\t"
        "ds_read_b64 v[48:49], v0 offset:32\n\t"
        "ds_read_b64 v[50:51], v0 offset:40\n\t"
        "v_mfma_f32_32x32x16_bf16 v[20:35], v[36:39], v[60:63], v[20:35]\n\t"
        "v_mfma_f32_32x32x16_bf16 v[4:19], v[36:39], v[52:55], v[4:19]\n\t"
        "v_mfma_f32_32x32x16_bf16 v[20:35], v[44:47], v[52:55], v[20:35]\n\t"
        "s_waitcnt lgkmcnt(0)\n\t"
        "ds_read_b64 v[36:37], v0 offset:2112\n\t"
        "ds_read_b64 v[38:39], v0 offset:2120\n\t"
        "ds_read_b128 v[60:63], v1 offset:2640\n\t"
        "ds_read_b128 v[52:55], v1 offset:80\n\t"
        "ds_read_b64 v[44:45], v0 offset:64\n\t"
        "ds_read_b64 v[46:47], v0 offset:72\n\t"
        "v_mfma_f32_32x32x16_bf16 v[20:35], v[40:43], v[64:67], v[20:35]\n\t"
        "v_mfma_f32_32x32x16_bf16 v[4:19], v[40:43], v[56:59], v[4:19]\n\t"
        "v_mfma_f32_32x32x16_bf16 v[20:35], v[48:51], v[56:59], v[20:35]\n\t"
        "s_waitcnt lgkmcnt(0)\n\t"
        "ds_read_b64 v[40:41], v0 offset:2144\n\t"
        "ds_read_b64 v[42:43], v0 offset:2152\n\t"
        "ds_read_b128 v[64:67], v1 offset:2672\n\t"
        "ds_read_b128 v[56:59], v1 offset:112\n\t"
        "ds_read_b64 v[48:49], v0 offset:96\n\t"
        "ds_read_b64 v[50:51], v0 offset:104\n\t"
        "v_mfma_f32_32x32x16_bf16 v[20:35], v[36:39], v[60:63], v[20:35]\n\t"
        "v_mfma_f32_32x32x16_bf16 v[4:19], v[36:39], v[52:55], v[4:19]\n\t"
        "v_mfma_f32_32x32x16_bf16 v[20:35], v[44:47], v[52:55], v[20:35]\n\t"
        "s_waitcnt lgkmcnt(0)\n\t"
        "ds_read_b64 v[36:37], v0 offset:2176\n\t"
        "ds_read_b64 v[38:39], v0 offset:2184\n\t"
        "ds_read_b128 v[60:63], v1 offset:2720\n\t"
        "ds_read_b128 v[52:55], v1 offset:160\n\t"
        "ds_read_b64 v[44:45], v0 offset:128\n\t"
        "ds_read_b64 v[46:47], v0 offset:136\n\t"
        "v_mfma_f32_32x32x16_bf16 v[20:35], v[40:43], v[64:67], v[20:35]\n\t"
        "v_mfma_f32_32x32x16_bf16 v[4:19], v[40:43], v[56:59], v[4:19]\n\t"
        "v_mfma_f32_32x32x16_bf16 v[20:35], v[48:51], v[56:59], v[20:35]\n\t"
        "s_waitcnt lgkmcnt(0)\n\t"
        "ds_read_b64 v[40:41], v0 offset:2208\n\t"
        "ds_read_b64 v[42:43], v0 offset:2216\n\t"
        "ds_read_b128 v[64:67], v1 offset:2752\n\t"
        "ds_read_b128 v[56:59], v1 offset:192\n\t"
        "ds_read_b64 v[48:49], v0 offset:160\n\t"
        "ds_read_b64 v[50:51], v0 offset:168\n\t"
        "v_mfma_f32_32x32x16_bf16 v[20:35], v[36:39], v[60:63], v[20:35]\n\t"
        "v_mfma_f32_32x32x16_bf16 v[4:19], v[36:39], v[52:55], v[4:19]\n\t"
        "v_mfma_f32_32x32x16_bf16 v[20:35], v[44:47], v[52:55], v[20:35]\n\t"
        "s_waitcnt lgkmcnt(0)\n\t"
        "ds_read_b64 v[36:37], v0 offset:2240\n\t"
        "ds_read_b64 v[38:39], v0 offset:2248\n\t"
        "ds_read_b128 v[60:63], v1 offset:2800\n\t"
        "ds_read_b128 v[52:55], v1 offset:240\n\t"
        "ds_read_b64 v[44:45], v0 offset:192\n\t"
        "ds_read_b64 v[46:47], v0 offset:200\n\t"
        "v_mfma_f32_32x32x16_bf16 v[20:35], v[40:43], v[64:67], v[20:35]\n\t"
        "v_mfma_f32_32x32x16_bf16 v[4:19], v[40:43], v[56:59], v[4:19]\n\t"
        "v_mfma_f32_32x32x16_bf16 v[20:35], v[48:51], v[56:59], v[20:35]\n\t"
        "s_waitcnt lgkmcnt(0)\n\t"
        "ds_read_b64 v[40:41], v0 offset:2272\n\t"
        "ds_read_b64 v[42:43], v0 offset:2280\n\t"
        "ds_read_b128 v[64:67], v1 offset:2832\n\t"
        "ds_read_b128 v[56:59], v1 offset:272\n\t"
        "ds_read_b64 v[48:49], v0 offset:224\n\t"
        "ds_read_b64 v[50:51], v0 offset:232\n\t"
        "v_mfma_f32_32x32x16_bf16 v[20:35], v[36:39], v[60:63], v[20:35]\n\t"
        "v_mfma_f32_32x32x16_bf16 v[4:19], v[36:39], v[52:55], v[4:19]\n\t"
        "v_mfma_f32_32x32x16_bf16 v[20:35], v[44:47], v[52:55], v[20:35]\n\t"
        "s_waitcnt lgkmcnt(0)\n\t"
        "ds_read_b64 v[36:37], v0 offset:2304\n\t"
        "ds_read_b64 v[38:39], v0 offset:2312\n\t"
        "ds_read_b128 v[60:63], v1 offset:2880\n\t"
        "ds_read_b128 v[52:55], v1 offset:320\n\t"
        "ds_read_b64 v[44:45], v0 offset:256\n\t"
        "ds_read_b64 v[46:47], v0 offset:264\n\t"
        "v_mfma_f32_32x32x16_bf16 v[20:35], v[40:43], v[64:67], v[20:35]\n\t"
        "v_mfma_f32_32x32x16_bf16 v[4:19], v[40:43], v[56:59], v[4:19]\n\t"
        "v_mfma_f32_32x32x16_bf16 v[20:35], v[48:51], v[56:59], v[20:35]\n\t"
        "s_waitcnt lgkmcnt(0)\n\t"
        "ds_read_b64 v[40:41], v0 offset:2336\n\t"
        "ds_read_b64 v[42:43], v0 offset:2344\n\t"
        "ds_read_b128 v[64:67], v1 offset:2912\n\t"
        "ds_read_b128 v[56:59], v1 offset:352\n\t"
        "ds_read_b64 v[48:49], v0 offset:288\n\t"
        "ds_read_b64 v[50:51], v0 offset:296\n\t"
        "v_mfma_f32_32x32x16_bf16 v[20:35], v[36:39], v[60:63], v[20:35]\n\t"
        "v_mfma_f32_32x32x16_bf16 v[4:19], v[36:39], v[52:55], v[4:19]\n\t"
        "v_mfma_f32_32x32x16_bf16 v[20:35], v[44:47], v[52:55], v[20:35]\n\t"
        "s_waitcnt lgkmcnt(0)\n\t"
        "ds_read_b64 v[36:37], v0 offset:2368\n\t"
        "ds_read_b64 v[38:39], v0 offset:2376\n\t"
        "ds_read_b128 v[60:63], v1 offset:2960\n\t"
        "ds_read_b128 v[52:55], v1 offset:400\n\t"
        "ds_read_b64 v[44:45], v0 offset:320\n\t"
        "ds_read_b64 v[46:47], v0 offset:328\n\t"
        "v_mfma_f32_32x32x16_bf16 v[20:35], v[40:43], v[64:67], v[20:35]\n\t"
        "v_mfma_f32_32x32x16_bf16 v[4:19], v[40:43], v[56:59], v[4:19]\n\t"
        "v_mfma_f32_32x32x16_bf16 v[20:35], v[48:51], v[56:59], v[20:35]\n\t"
        "s_waitcnt lgkmcnt(0)\n\t"
        "ds_read_b64 v[40:41], v0 offset:2400\n\t"
        "ds_read_b64 v[42:43], v0 offset:2408\n\t"
        "ds_read_b128 v[64:67], v1 offset:2992\n\t"
        "ds_read_b128 v[56:59], v1 offset:432\n\t"
        "ds_read_b64 v[48:49], v0 offset:352\n\t"
        "ds_read_b64 v[50:51], v0 offset:360\n\t"
        "v_mfma_f32_32x32x16_bf16 v[20:35], v[36:39], v[60:63], v[20:35]\n\t"
        "v_mfma_f32_32x32x16_bf16 v[4:19], v[36:39], v[52:55], v[4:19]\n\t"
        "v_mfma_f32_32x32x16_bf16 v[20:35], v[44:47], v[52:55], v[20:35]\n\t"
        "s_waitcnt lgkmcnt(0)\n\t"
        "ds_read_b64 v[36:37], v0 offset:2432\n\t"
        "ds_read_b64 v[38:39], v0 offset:2440\n\t"
        "ds_read_b128 v[60:63], v1 offset:3040\n\t"
        "ds_read_b128 v[52:55], v1 offset:480\n\t"
        "ds_read_b64 v[44:45], v0 offset:384\n\t"
        "ds_read_b64 v[46:47], v0 offset:392\n\t"
        "v_mfma_f32_32x32x16_bf16 v[20:35], v[40:43], v[64:67], v[20:35]\n\t"
        "v_mfma_f32_32x32x16_bf16 v[4:19], v[40:43], v[56:59], v[4:19]\n\t"
        "v_mfma_f32_32x32x16_bf16 v[20:35], v[48:51], v[56:59], v[20:35]\n\t"
        "s_waitcnt lgkmcnt(0)\n\t"
        "ds_read_b64 v[40:41], v0 offset:2464\n\t"
        "ds_read_b64 v[42:43], v0 offset:2472\n\t"
        "ds_read_b128 v[64:67], v1 offset:3072\n\t"
        "ds_read_b128 v[56:59], v1 offset:512\n\t"
        "ds_read_b64 v[48:49], v0 offset:416\n\t"
        "ds_read_b64 v[50:51], v0 offset:424\n\t"
        "v_mfma_f32_32x32x16_bf16 v[20:35], v[36:39], v[60:63], v[20:35]\n\t"
        "v_mfma_f32_32x32x16_bf16 v[4:19], v[36:39], v[52:55], v[4:19]\n\t"
        "v_mfma_f32_32x32x16_bf16 v[20:35], v[44:47], v[52:55], v[20:35]\n\t"
        "s_waitcnt lgkmcnt(0)\n\t"
        "ds_read_b64 v[36:37], v0 offset:2496\n\t"
        "ds_read_b64 v[38:39], v0 offset:2504\n\t"
        "ds_read_b128 v[60:63], v1 offset:3120\n\t"
        "ds_read_b128 v[52:55], v1 offset:560\n\t"
        "ds_read_b64 v[44:45], v0 offset:448\n\t"
        "ds_read_b64 v[46:47], v0 offset:456\n\t"
        "v_mfma_f32_32x32x16_bf16 v[20:35], v[40:43], v[64:67], v[20:35]\n\t"
        "v_mfma_f32_32x32x16_bf16 v[4:19], v[40:43], v[56:59], v[4:19]\n\t"
        "v_mfma_f32_32x32x16_bf16 v[20:35], v[48:51], v[56:59], v[20:35]\n\t"
        "s_waitcnt lgkmcnt(0)\n\t"
        "ds_read_b64 v[40:41], v0 offset:2528\n\t"
        "ds_read_b64 v[42:43], v0 offset:2536\n\t"
        "ds_read_b128 v[64:67], v1 offset:3152\n\t"
        "ds_read_b128 v[56:59], v1 offset:592\n\t"
        "ds_read_b64 v[48:49], v0 offset:480\n\t"
        "ds_read_b64 v[50:51], v0 offset:488\n\t"
        "v_mfma_f32_32x32x16_bf16 v[20:35], v[36:39], v[60:63], v[20:35]\n\t"
        "v_mfma_f32_32x32x16_bf16 v[4:19], v[36:39], v[52:55], v[4:19]\n\t"
        "v_mfma_f32_32x32x16_bf16 v[20:35], v[44:47], v[52:55], v[20:35]\n\t"
        "s_waitcnt lgkmcnt(0)\n\t"
        "v_mfma_f32_32x32x16_bf16 v[20:35], v[40:43], v[64:67], v[20:35]\n\t"
        "v_mfma_f32_32x32x16_bf16 v[4:19], v[40:43], v[56:59], v[4:19]\n\t"
        "v_mfma_f32_32x32x16_bf16 v[20:35], v[48:51], v[56:59], v[20:35]\n\t"
        "s_nop 7\n\t"
        "s_nop 7\n\t"
        : "={v[4:19]}"(y0), "={v[20:35]}"(y1)
        : "{v0}"(aA), "{v1}"(aB)
        : "v36", "v37", "v38", "v39", "v40", "v41", "v42", "v43", "v44", "v45", "v46", "v47", "v48", "v49", "v50", "v51", "v52", "v53", "v54", "v55", "v56", "v57", "v58", "v59", "v60", "v61", "v62", "v63", "v64", "v65", "v66", "v67", "memory");
}
__device__ __forceinline__ void conv_tail(f32x16& y0, f32x16& y1, unsigned aA, unsigned aB) {
    asm volatile(
        "ds_read_b64 v[36:37], v0 offset:2048\n\t"
        "ds_read_b64 v[38:39], v0 offset:2056\n\t"
        "ds_read_b128 v[60:63], v1 offset:2560\n\t"
        "ds_read_b128 v[52:55], v1 offset:0\n\t"
        "ds_read_b64 v[44:45], v0 offset:0\n\t"
        "ds_read_b64 v[46:47], v0 offset:8\n\t"
        "v_mov_b32 v4, 0\n\t"
        "v_mov_b32 v5, 0\n\t"
        "v_mov_b32 v6, 0\n\t"
        "v_mov_b32 v7, 0\n\t"
        "v_mov_b32 v8, 0\n\t"
        "v_mov_b32 v9, 0\n\t"
        "v_mov_b32 v10, 0\n\t"
        "v_mov_b32 v11, 0\n\t"
        "v_mov_b32 v12, 0\n\t"
        "v_mov_b32 v13, 0\n\t"
        "v_mov_b32 v14, 0\n\t"
        "v_mov_b32 v15, 0\n\t"
        "v_mov_b32 v16, 0\n\t"
        "v_mov_b32 v17, 0\n\t"
        "v_mov_b32 v18, 0\n\t"
        "v_mov_b32 v19, 0\n\t"
        "v_mov_b32 v20, 0\n\t"
        "v_mov_b32 v21, 0\n\t"
        "v_mov_b32 v22, 0\n\t"
        "v_mov_b32 v23, 0\n\t"
        "v_mov_b32 v24, 0\n\t"
        "v_mov_b32 v25, 0\n\t"
        "v_mov_b32 v26, 0\n\t"
        "v_mov_b32 v27, 0\n\t"
        "v_mov_b32 v28, 0\n\t"
        "v_mov_b32 v29, 0\n\t"
        "v_mov_b32 v30, 0\n\t"
        "v_mov_b32 v31, 0\n\t"
        "v_mov_b32 v32, 0\n\t"
        "v_mov_b32 v33, 0\n\t"
        "v_mov_b32 v34, 0\n\t"
        "v_mov_b32 v35, 0\n\t"
        "s_waitcnt lgkmcnt(0)\n\t"
        "ds_read_b64 v[40:41], v0 offset:2080\n\t"
        "ds_read_b64 v[42:43], v0 offset:2088\n\t"
        "ds_read_b128 v[64:67], v1 offset:2592\n\t"
        "ds_read_b128 v[56:59], v1 offset:32\n\t"
        "ds_read_b64 v[48:49], v0 offset:32\n\t"
        "ds_read_b64 v[50:51], v0 offset:40\n\t"
        "v_mfma_f32_32x32x16_bf16 v[20:35], v[36:39], v[60:63], v[20:35]\n\t"
        "v_mfma_f32_32x32x16_bf16 v[4:19], v[36:39], v[52:55], v[4:19]\n\t"
        "v_mfma_f32_32x32x16_bf16 v[20:35], v[44:47], v[52:55], v[20:35]\n\t"
        "s_waitcnt lgkmcnt(0)\n\t"
        "ds_read_b64 v[36:37], v0 offset:2112\n\t"
        "ds_read_b64 v[38:39], v0 offset:2120\n\t"
        "ds_read_b128 v[60:63], v1 offset:2640\n\t"
        "ds_read_b128 v[52:55], v1 offset:80\n\t"
        "ds_read_b64 v[44:45], v0 offset:64\n\t"
        "ds_read_b64 v[46:47], v0 offset:72\n\t"
        "v_mfma_f32_32x32x16_bf16 v[20:35], v[40:43], v[64:67], v[20:35]\n\t"
        "v_mfma_f32_32x32x16_bf16 v[4:19], v[40:43], v[56:59], v[4:19]\n\t"
        "v_mfma_f32_32x32x16_bf16 v[20:35], v[48:51], v[56:59], v[20:35]\n\t"
        "s_waitcnt lgkmcnt(0)\n\t"
        "ds_read_b64 v[40:41], v0 offset:2144\n\t"
        "ds_read_b64 v[42:43], v0 offset:2152\n\t"
        "ds_read_b128 v[64:67], v1 offset:2672\n\t"
        "ds_read_b128 v[56:59], v1 offset:112\n\t"
        "ds_read_b64 v[48:49], v0 offset:96\n\t"
        "ds_read_b64 v[50:51], v0 offset:104\n\t"
        "v_mfma_f32_32x32x16_bf16 v[20:35], v[36:39], v[60:63], v[20:35]\n\t"
        "v_mfma_f32_32x32x16_bf16 v[4:19], v[36:39], v[52:55], v[4:19]\n\t"
        "v_mfma_f32_32x32x16_bf16 v[20:35], v[44:47], v[52:55], v[20:35]\n\t"
        "s_waitcnt lgkmcnt(0)\n\t"
        "ds_read_b64 v[36:37], v0 offset:2176\n\t"
        "ds_read_b64 v[38:39], v0 offset:2184\n\t"
        "ds_read_b128 v[60:63], v1 offset:2720\n\t"
        "ds_read_b128 v[52:55], v1 offset:160\n\t"
        "ds_read_b64 v[44:45], v0 offset:128\n\t"
        "ds_read_b64 v[46:47], v0 offset:136\n\t"
        "v_mfma_f32_32x32x16_bf16 v[20:35], v[40:43], v[64:67], v[20:35]\n\t"
        "v_mfma_f32_32x32x16_bf16 v[4:19], v[40:43], v[56:59], v[4:19]\n\t"
        "v_mfma_f32_32x32x16_bf16 v[20:35], v[48:51], v[56:59], v[20:35]\n\t"
        "s_waitcnt lgkmcnt(0)\n\t"
        "ds_read_b64 v[40:41], v0 offset:2208\n\t"
        "ds_read_b64 v[42:43], v0 offset:2216\n\t"
        "ds_read_b128 v[64:67], v1 offset:2752\n\t"
        "ds_read_b128 v[56:59], v1 offset:192\n\t"
        "ds_read_b64 v[48:49], v0 offset:160\n\t"
        "ds_read_b64 v[50:51], v0 offset:168\n\t"
        "v_mfma_f32_32x32x16_bf16 v[20:35], v[36:39], v[60:63], v[20:35]\n\t"
        "v_mfma_f32_32x32x16_bf16 v[4:19], v[36:39], v[52:55], v[4:19]\n\t"
        "v_mfma_f32_32x32x16_bf16 v[20:35], v[44:47], v[52:55], v[20:35]\n\t"
        "s_waitcnt lgkmcnt(0)\n\t"
        "ds_read_b64 v[36:37], v0 offset:2240\n\t"
        "ds_read_b64 v[38:39], v0 offset:2248\n\t"
        "ds_read_b128 v[60:63], v1 offset:2800\n\t"
        "ds_read_b128 v[52:55], v1 offset:240\n\t"
        "ds_read_b64 v[44:45], v0 offset:192\n\t"
        "ds_read_b64 v[46:47], v0 offset:200\n\t"
        "v_mfma_f32_32x32x16_bf16 v[20:35], v[40:43], v[64:67], v[20:35]\n\t"
        "v_mfma_f32_32x32x16_bf16 v[4:19], v[40:43], v[56:59], v[4:19]\n\t"
        "v_mfma_f32_32x32x16_bf16 v[20:35], v[48:51], v[56:59], v[20:35]\n\t"
        "s_waitcnt lgkmcnt(0)\n\t"
        "ds_read_b64 v[40:41], v0 offset:2272\n\t"
        "ds_read_b64 v[42:43], v0 offset:2280\n\t"
        "ds_read_b128 v[64:67], v1 offset:2832\n\t"
        "ds_read_b128 v[56:59], v1 offset:272\n\t"
        "ds_read_b64 v[48:49], v0 offset:224\n\t"
        "ds_read_b64 v[50:51], v0 offset:232\n\t"
        "v_mfma_f32_32x32x16_bf16 v[20:35], v[36:39], v[60:63], v[20:35]\n\t"
        "v_mfma_f32_32x32x16_bf16 v[4:19], v[36:39], v[52:55], v[4:19]\n\t"
        "v_mfma_f32_32x32x16_bf16 v[20:35], v[44:47], v[52:55], v[20:35]\n\t"
        "s_waitcnt lgkmcnt(0)\n\t"
        "ds_read_b64 v[36:37], v0 offset:2304\n\t"
        "ds_read_b64 v[38:39], v0 offset:2312\n\t"
        "ds_read_b128 v[60:63], v1 offset:2880\n\t"
        "ds_read_b128 v[52:55], v1 offset:320\n\t"
        "ds_read_b64 v[44:45], v0 offset:256\n\t"
        "ds_read_b64 v[46:47], v0 offset:264\n\t"
        "v_mfma_f32_32x32x16_bf16 v[20:35], v[40:43], v[64:67], v[20:35]\n\t"
        "v_mfma_f32_32x32x16_bf16 v[4:19], v[40:43], v[56:59], v[4:19]\n\t"
        "v_mfma_f32_32x32x16_bf16 v[20:35], v[48:51], v[56:59], v[20:35]\n\t"
        "s_waitcnt lgkmcnt(0)\n\t"
        "ds_read_b64 v[40:41], v0 offset:2336\n\t"
        "ds_read_b64 v[42:43], v0 offset:2344\n\t"
        "ds_read_b128 v[64:67], v1 offset:2912\n\t"
        "ds_read_b128 v[56:59], v1 offset:352\n\t"
        "ds_read_b64 v[48:49], v0 offset:288\n\t"
        "ds_read_b64 v[50:51], v0 offset:296\n\t"
        "v_mfma_f32_32x32x16_bf16 v[20:35], v[36:39], v[60:63], v[20:35]\n\t"
        "v_mfma_f32_32x32x16_bf16 v[4:19], v[36:39], v[52:55], v[4:19]\n\t"
        "v_mfma_f32_32x32x16_bf16 v[20:35], v[44:47], v[52:55], v[20:35]\n\t"
        "s_waitcnt lgkmcnt(0)\n\t"
        "ds_read_b64 v[36:37], v0 offset:2368\n\t"
        "ds_read_b64 v[38:39], v0 offset:2376\n\t"
        "ds_read_b128 v[60:63], v1 offset:2960\n\t"
        "ds_read_b128 v[52:55], v1 offset:400\n\t"
        "ds_read_b64 v[44:45], v0 offset:320\n\t"
        "ds_read_b64 v[46:47], v0 offset:328\n\t"
        "v_mfma_f32_32x32x16_bf16 v[20:35], v[40:43], v[64:67], v[20:35]\n\t"
        "v_mfma_f32_32x32x16_bf16 v[4:19], v[40:43], v[56:59], v[4:19]\n\t"
        "v_mfma_f32_32x32x16_bf16 v[20:35], v[48:51], v[56:59], v[20:35]\n\t"
        "s_waitcnt lgkmcnt(0)\n\t"
        "ds_read_b64 v[40:41], v0 offset:2400\n\t"
        "ds_read_b64 v[42:43], v0 offset:2408\n\t"
        "ds_read_b128 v[64:67], v1 offset:2992\n\t"
        "ds_read_b128 v[56:59], v1 offset:432\n\t"
        "ds_read_b64 v[48:49], v0 offset:352\n\t"
        "ds_read_b64 v[50:51], v0 offset:360\n\t"
        "v_mfma_f32_32x32x16_bf16 v[20:35], v[36:39], v[60:63], v[20:35]\n\t"
        "v_mfma_f32_32x32x16_bf16 v[4:19], v[36:39], v[52:55], v[4:19]\n\t"
        "v_mfma_f32_32x32x16_bf16 v[20:35], v[44:47], v[52:55], v[20:35]\n\t"
        "s_waitcnt lgkmcnt(0)\n\t"
        "ds_read_b64 v[36:37], v0 offset:2432\n\t"
        "ds_read_b64 v[38:39], v0 offset:2440\n\t"
        "ds_read_b128 v[60:63], v1 offset:3040\n\t"
        "ds_read_b128 v[52:55], v1 offset:480\n\t"
        "ds_read_b64 v[44:45], v0 offset:384\n\t"
        "ds_read_b64 v[46:47], v0 offset:392\n\t"
        "v_mfma_f32_32x32x16_bf16 v[20:35], v[40:43], v[64:67], v[20:35]\n\t"
        "v_mfma_f32_32x32x16_bf16 v[4:19], v[40:43], v[56:59], v[4:19]\n\t"
        "v_mfma_f32_32x32x16_bf16 v[20:35], v[48:51], v[56:59], v[20:35]\n\t"
        "s_waitcnt lgkmcnt(0)\n\t"
        "ds_read_b64 v[40:41], v0 offset:2464\n\t"
        "ds_read_b64 v[42:43], v0 offset:2472\n\t"
        "ds_read_b128 v[64:67], v1 offset:3072\n\t"
        "ds_read_b128 v[56:59], v1 offset:512\n\t"
        "ds_read_b64 v[48:49], v0 offset:416\n\t"
        "ds_read_b64 v[50:51], v0 offset:424\n\t"
        "v_mfma_f32_32x32x16_bf16 v[20:35], v[36:39], v[60:63], v[20:35]\n\t"
        "v_mfma_f32_32x32x16_bf16 v[4:19], v[36:39], v[52:55], v[4:19]\n\t"
        "v_mfma_f32_32x32x16_bf16 v[20:35], v[44:47], v[52:55], v[20:35]\n\t"
        "s_waitcnt lgkmcnt(0)\n\t"
        "ds_read_b64 v[36:37], v0 offset:2496\n\t"
        "ds_read_b64 v[38:39], v0 offset:2504\n\t"
        "ds_read_b128 v[60:63], v1 offset:3120\n\t"
        "ds_read_b128 v[52:55], v1 offset:560\n\t"
        "ds_read_b64 v[44:45], v0 offset:448\n\t"
        "ds_read_b64 v[46:47], v0 offset:456\n\t"
        "v_mfma_f32_32x32x16_bf16 v[20:35], v[40:43], v[64:67], v[20:35]\n\t"
        "v_mfma_f32_32x32x16_bf16 v[4:19], v[40:43], v[56:59], v[4:19]\n\t"
        "v_mfma_f32_32x32x16_bf16 v[20:35], v[48:51], v[56:59], v[20:35]\n\t"
        "s_waitcnt lgkmcnt(0)\n\t"
        "ds_read_b64 v[40:41], v0 offset:2528\n\t"
        "ds_read_b64 v[42:43], v0 offset:2536\n\t"
        "ds_read_b128 v[64:67], v1 offset:3152\n\t"
        "ds_read_b128 v[56:59], v1 offset:592\n\t"
        "ds_read_b64 v[48:49], v0 offset:480\n\t"
        "ds_read_b64 v[50:51], v0 offset:488\n\t"
        "v_mfma_f32_32x32x16_bf16 v[20:35], v[36:39], v[60:63], v[20:35]\n\t"
        "v_mfma_f32_32x32x16_bf16 v[4:19], v[36:39], v[52:55], v[4:19]\n\t"
        "v_mfma_f32_32x32x16_bf16 v[20:35], v[44:47], v[52:55], v[20:35]\n\t"
        "s_waitcnt lgkmcnt(0)\n\t"
        "ds_read_b64 v[36:37], v0 offset:2560\n\t"
        "ds_read_b64 v[38:39], v0 offset:2568\n\t"
        "ds_read_b128 v[60:63], v1 offset:3200\n\t"
        "ds_read_b128 v[52:55], v1 offset:640\n\t"
        "v_mfma_f32_32x32x16_bf16 v[20:35], v[40:43], v[64:67], v[20:35]\n\t"
        "v_mfma_f32_32x32x16_bf16 v[4:19], v[40:43], v[56:59], v[4:19]\n\t"
        "v_mfma_f32_32x32x16_bf16 v[20:35], v[48:51], v[56:59], v[20:35]\n\t"
        "s_waitcnt lgkmcnt(0)\n\t"
        "ds_read_b64 v[40:41], v0 offset:2592\n\t"
        "ds_read_b64 v[42:43], v0 offset:2600\n\t"
        "ds_read_b128 v[64:67], v1 offset:3232\n\t"
        "ds_read_b128 v[56:59], v1 offset:672\n\t"
        "v_mfma_f32_32x32x16_bf16 v[20:35], v[36:39], v[60:63], v[20:35]\n\t"
        "v_mfma_f32_32x32x16_bf16 v[4:19], v[36:39], v[52:55], v[4:19]\n\t"
        "s_waitcnt lgkmcnt(0)\n\t"
        "v_mfma_f32_32x32x16_bf16 v[20:35], v[40:43], v[64:67], v[20:35]\n\t"
        "v_mfma_f32_32x32x16_bf16 v[4:19], v[40:43], v[56:59], v[4:19]\n\t"
        "s_nop 7\n\t"
        "s_nop 7\n\t"
        : "={v[4:19]}"(y0), "={v[20:35]}"(y1)
        : "{v0}"(aA), "{v1}"(aB)
        : "v36", "v37", "v38", "v39", "v40", "v41", "v42", "v43", "v44", "v45", "v46", "v47", "v48", "v49", "v50", "v51", "v52", "v53", "v54", "v55", "v56", "v57", "v58", "v59", "v60", "v61", "v62", "v63", "v64", "v65", "v66", "v67", "memory");
}

__device__ __forceinline__ void c2_conv(int w, int ch, int cs, int kq, int nwaves, int b, const u16* __restrict__ ssmk, const u16* __restrict__ ssmD, char* __restrict__ ws, rsrc_t R,
                                        unsigned lds0, unsigned epoch STARG) {
    const int lane = lane_id(), r = lane & 31, hi = lane >> 5;
    const unsigned L = lds0 + cs * CV_SZ, cnt = lds0 + L_SYNC + (SY_CV + 4 * cs) * 4;
    if (b < 224) lds_wait(lds0 + L_SYNC + (SY_RING + 17) * 4, 4);
    const u16* hg = ssmk + (size_t)ch * SEQ;
    for (int i = kq; i < 5; i += 4) {
        const int sg = i * 64 + lane, blk = sg / 5, pc = sg % 5;
        glds16(hg + blk * 32 + (pc < 3 ? pc : 3) * 8, rfl(L + CV_HB + 2560 + i * 1024));
    }
    if (kq < 3) { const int o = kq * 64 + lane; if (o < 160) ldswr<u32x4>(L + CV_HB + o * 16, u32x4{0, 0, 0, 0}); }
    const float Dv = bflo((unsigned)ssmD[ch]);
    if (kq == 0) {
        poll(ws, FL_C1 + ((ch >> 6) * 32 + (lane & 31)) * 4, lane < 32, 0, false, epoch);
        if (lane == 0) ldswr<unsigned>(cnt + 8, 1u);
    } else lds_wait(cnt + 8, 1);
    ST(6);
    const char* xp = ws + WS_XP + (size_t)ch * (XPS * 2) + kq * 2;
#pragma unroll
    for (int i = 0; i < 4; ++i) glds16_sc1(xp + i * 1024 + lane * 16, rfl(L + kq * CV_A + i * 1024));
    if (lane < 12) glds16_sc1(xp + 4096 + lane * 16, rfl(L + kq * CV_A + 4096));
    WAITV0();
    sw_barrier(cnt, 4);
    ST(7);
    const int beta = r >> 2, cpy = (3 - r) & 3;
    const unsigned aA = L + cpy * CV_A + (28 - 4 * beta + 8 * hi) * 2 + kq * 16 * 32;
    const unsigned aB = L + CV_HB + r * 80 + hi * 16 + kq * 8 * 80;
    const int half = kq >> 1, gp = kq & 1;
    u32x2 kvp[2];
#pragma unroll
    for (int gg = 0; gg < 2; ++gg) kvp[gg] = ldsrd<u32x2>(L + (2076 - (half * 1024 + 32 * r + 8 * (2 * gp + gg) + 4 * hi)) * 2);
    f32x16 y0, y1;
    if (kq == 3) conv_tail(y0, y1, aA, aB); else conv_body(y0, y1, aA, aB);
    ST(8);
    sw_barrier(cnt, 8);
#pragma unroll
    for (int c = 0; c < 4; ++c) {
        if (c != kq) {
            const f32x16& y = (c >> 1) ? y1 : y0;
            const int o = (c & 1) * 8;
            ldswr<f32x4>(L + (c * 3 + (kq > c ? kq - 1 : kq)) * 2048 + lane * 16, f32x4{y[o], y[o + 1], y[o + 2], y[o + 3]});
            ldswr<f32x4>(L + (c * 3 + (kq > c ? kq - 1 : kq)) * 2048 + 1024 + lane * 16, f32x4{y[o + 4], y[o + 5], y[o + 6], y[o + 7]});
        }
    }
    sw_barrier(cnt + 4, 4);
    float v[8];
    {
        const f32x16& y = half ? y1 : y0;
#pragma unroll
        for (int e = 0; e < 8; ++e) v[e] = gp ? y[8 + e] : y[e];
#pragma unroll
        for (int q = 0; q < 3; ++q) {
            const f32x4 a = ldsrd<f32x4>(L + (kq * 3 + q) * 2048 + lane * 16), c = ldsrd<f32x4>(L + (kq * 3 + q) * 2048 + 1024 + lane * 16);
#pragma unroll
            for (int e = 0; e < 4; ++e) { v[e] += a[e]; v[4 + e] += c[e]; }
        }
    }
    u32x2 o[2];
#pragma unroll
    for (int gg = 0; gg < 2; ++gg) {
        const float kv0 = bfhi(kvp[gg][1]), kv1 = bflo(kvp[gg][1]), kv2 = bfhi(kvp[gg][0]), kv3 = bflo(kvp[gg][0]);
        const float z0 = rbf(v[4 * gg]) + rbf(kv0 * Dv), z1 = rbf(v[4 * gg + 1]) + rbf(kv1 * Dv), z2 = rbf(v[4 * gg + 2]) + rbf(kv2 * Dv), z3 = rbf(v[4 * gg + 3]) + rbf(kv3 * Dv);
        o[gg] = u32x2{cvtpk(z0, z1), cvtpk(z2, z3)};
    }
    const auto s0 = __builtin_amdgcn_permlane32_swap(o[0][0], o[1][0], false, false);
    const auto s1 = __builtin_amdgcn_permlane32_swap(o[0][1], o[1][1], false, false);
    st128(R, WS_YT + (ch * SEQ + half * 1024 + 32 * r + 8 * (2 * gp + hi)) * 2, u32x4{s0[0], s1[0], s0[1], s1[1]});
    publish(ws, FL_C2 + b * 4, lds0 + L_SYNC + SY_C2 * 4, nwaves, epoch);
    ST(9);
}

__device__ __forceinline__ void x_tail(int b, int l, int nconv, const u16* __restrict__ wo, const u16* __restrict__ bq, char* __restrict__ ws, rsrc_t R, u16* __restrict__ out,
                                       unsigned lds0, unsigned epoch STARG) {
    const int w = l + 8;
    const int lane = lane_id(), t8 = l * 64 + lane;
    const int xcd = b & 7, idx = b >> 3;
    const int tb = (xcd >> 1) * 8 + (idx & 7), cg = (xcd & 1) * 4 + (idx >> 3);
    const int t0 = tb * 64, n0 = cg * 72;
    u32x4 qv[2], yv[2];
#pragma unroll
    for (int k = 0; k < 2; ++k) { const int it = t8 + 512 * k; if (it < 576) qv[k] = *(const u32x4*)(bq + (size_t)(t0 + it / 9) * DM + n0 + (it % 9) * 8); }
    lds_wait(lds0 + L_SYNC + SY_C2 * 4, nconv);
    for (int p = l; p < 81; p += 8) {
        const int sg = p * 64 + lane, kp = sg / 9, c = sg - 9 * kp;
        int k;
        if (kp < 512) { const int hh = kp >> 4, pos = kp & 15, fi = (pos & 3) + 4 * (pos >> 3) + 8 * ((pos >> 2) & 1); k = 18 * hh + ((hh & 1) ? fi + 2 : fi); }
        else { const int hh = (kp - 512) >> 1, e = kp & 1; k = 18 * hh + ((hh & 1) ? e : 16 + e); }
        glds16((const char*)wo + (size_t)k * 1152 + n0 * 2 + c * 16, rfl(lds0 + p * 1024));
    }
    if (l == 0) {
        const int w0 = n0 < 448 ? n0 >> 1 : 224 + ((n0 - 448) >> 2), cl = n0 + 71, w1 = cl < 448 ? cl >> 1 : 224 + ((cl - 448) >> 2);
        poll(ws, FL_C2 + (w0 + lane) * 4, w0 + lane <= w1, 0, false, epoch);
        if (lane == 0) ldswr<unsigned>(lds0 + L_SYNC + SY_GO3 * 4, 1u);
    } else lds_wait(lds0 + L_SYNC + SY_GO3 * 4, 1);
    const unsigned ytl = lds0 + L_C3;
#pragma unroll
    for (int k = 0; k < 2; ++k) { const int it = t8 + 512 * k; if (it < 576) yv[k] = ld128(R, WS_YT + ((n0 + (it >> 3)) * SEQ + t0 + (it & 7) * 8) * 2); }
    WAITV0();
    if (lane == 0) lds_add(lds0 + L_SYNC + SY_WO * 4);
#pragma unroll
    for (int k = 0; k < 2; ++k) { const int it = t8 + 512 * k; if (it < 576) ldswr<u32x4>(ytl + (it >> 3) * 144 + (it & 7) * 16, yv[k]); }
    sw_barrier(lds0 + L_SYNC + SY_C3 * 4, 8);
#pragma unroll
    for (int k = 0; k < 2; ++k) {
        const int it = t8 + 512 * k;
        if (it < 576) {
            const int tok = it / 9, cc = it % 9;
            u32x4 o;
#pragma unroll
            for (int e = 0; e < 4; ++e) {
                const unsigned y0 = ldsrd<u16>(ytl + (cc * 8 + 2 * e) * 144 + tok * 2), y1 = ldsrd<u16>(ytl + (cc * 8 + 2 * e + 1) * 144 + tok * 2);
                o[e] = cvtpk(bflo(qv[k][e]) * bflo(y0), bfhi(qv[k][e]) * bflo(y1));
            }
            *(u32x4*)(out + (size_t)(t0 + tok) * 1152 + 576 + n0 + cc * 8) = o;
        }
    }
    ST(14);
}

__device__ __forceinline__ void attn_fast(f32x16 (&o)[2], const u32x4 (&q0)[2], const u32x2 (&q1)[2], unsigned aK0, unsigned aK1, unsigned aV,
                                          unsigned g0, unsigned g1, unsigned g2, unsigned m0, unsigned m1, unsigned m2, unsigned long long gb, unsigned cn) {
    unsigned sv, t;
    asm volatile(
        "s_mov_b32 %[sv], m0\n\t"
        "ds_read_b128 v[96:99], v12 offset:0\n\t"
        "ds_read_b64 v[120:121], v13 offset:0\n\t"
        "ds_read_b128 v[108:111], v12 offset:1024\n\t"
        "ds_read_b64 v[122:123], v13 offset:256\n\t"
        "ds_read_b128 v[100:103], v14 offset:0\n\t"
        "ds_read_b128 v[104:107], v14 offset:640\n\t"
        "v_mov_b32 v20, 0\n\t"
        "v_mov_b32 v21, 0\n\t"
        "v_mov_b32 v22, 0\n\t"
        "v_mov_b32 v23, 0\n\t"
        "v_mov_b32 v24, 0\n\t"
        "v_mov_b32 v25, 0\n\t"
        "v_mov_b32 v26, 0\n\t"
        "v_mov_b32 v27, 0\n\t"
        "v_mov_b32 v28, 0\n\t"
        "v_mov_b32 v29, 0\n\t"
        "v_mov_b32 v30, 0\n\t"
        "v_mov_b32 v31, 0\n\t"
        "v_mov_b32 v32, 0\n\t"
        "v_mov_b32 v33, 0\n\t"
        "v_mov_b32 v34, 0\n\t"
        "v_mov_b32 v35, 0\n\t"
        "v_mov_b32 v36, 0\n\t"
        "v_mov_b32 v37, 0\n\t"
        "v_mov_b32 v38, 0\n\t"
        "v_mov_b32 v39, 0\n\t"
        "v_mov_b32 v40, 0\n\t"
        "v_mov_b32 v41, 0\n\t"
        "v_mov_b32 v42, 0\n\t"
        "v_mov_b32 v43, 0\n\t"
        "v_mov_b32 v44, 0\n\t"
        "v_mov_b32 v45, 0\n\t"
        "v_mov_b32 v46, 0\n\t"
        "v_mov_b32 v47, 0\n\t"
        "v_mov_b32 v48, 0\n\t"
        "v_mov_b32 v49, 0\n\t"
        "v_mov_b32 v50, 0\n\t"
        "v_mov_b32 v51, 0\n\t"
        "s_waitcnt lgkmcnt(0)\n\t"
        "v_mfma_f32_32x32x16_bf16 v[52:67], v[96:99], v[0:3], 0\n\t"
        "v_mfma_f32_32x32x8_bf16 v[52:67], v[120:121], v[8:9], v[52:67]\n\t"
        "s_nop 7\n\t"
        "s_nop 7\n\t"
        "v_mfma_f32_32x32x16_bf16 v[68:83], v[96:99], v[4:7], 0\n\t"
        "v_exp_f32 v92, v52\n\t"
        "v_exp_f32 v93, v53\n\t"
        "v_exp_f32 v94, v54\n\t"
        "v_exp_f32 v95, v55\n\t"
        "v_cvt_pk_bf16_f32 v84, v92, v93\n\t"
        "v_cvt_pk_bf16_f32 v85, v94, v95\n\t"
        "v_mfma_f32_32x32x8_bf16 v[68:83], v[120:121], v[10:11], v[68:83]\n\t"
        "v_exp_f32 v92, v56\n\t"
        "v_exp_f32 v93, v57\n\t"
        "v_exp_f32 v94, v58\n\t"
        "v_exp_f32 v95, v59\n\t"
        "v_cvt_pk_bf16_f32 v86, v92, v93\n\t"
        "v_cvt_pk_bf16_f32 v87, v94, v95\n\t"
        "v_exp_f32 v92, v60\n\t"
        "v_exp_f32 v93, v61\n\t"
        "v_exp_f32 v94, v62\n\t"
        "v_exp_f32 v95, v63\n\t"
        "v_cvt_pk_bf16_f32 v88, v92, v93\n\t"
        "v_cvt_pk_bf16_f32 v89, v94, v95\n\t"
        "v_mfma_f32_32x32x16_bf16 v[20:35], v[100:103], v[84:87], v[20:35]\n\t"
        "v_exp_f32 v92, v64\n\t"
        "v_exp_f32 v93, v65\n\t"
        "v_exp_f32 v94, v66\n\t"
        "v_exp_f32 v95, v67\n\t"
        "v_cvt_pk_bf16_f32 v90, v92, v93\n\t"
        "v_cvt_pk_bf16_f32 v91, v94, v95\n\t"
        "ds_read_b128 v[96:99], v12 offset:2048\n\t"
        "ds_read_b64 v[120:121], v13 offset:512\n\t"
        "ds_read_b128 v[112:115], v14 offset:1280\n\t"
        "ds_read_b128 v[116:119], v14 offset:1920\n\t"
        "s_setprio 1\n\t"
        "v_mfma_f32_32x32x16_bf16 v[52:67], v[108:111], v[0:3], 0\n\t"
        "v_exp_f32 v92, v68\n\t"
        "v_exp_f32 v93, v69\n\t"
        "v_exp_f32 v94, v70\n\t"
        "v_exp_f32 v95, v71\n\t"
        "v_cvt_pk_bf16_f32 v84, v92, v93\n\t"
        "v_cvt_pk_bf16_f32 v85, v94, v95\n\t"
        "v_mfma_f32_32x32x8_bf16 v[52:67], v[122:123], v[8:9], v[52:67]\n\t"
        "v_exp_f32 v92, v72\n\t"
        "v_exp_f32 v93, v73\n\t"
        "v_exp_f32 v94, v74\n\t"
        "v_exp_f32 v95, v75\n\t"
        "v_cvt_pk_bf16_f32 v86, v92, v93\n\t"
        "v_cvt_pk_bf16_f32 v87, v94, v95\n\t"
        "v_mfma_f32_32x32x16_bf16 v[20:35], v[104:107], v[88:91], v[20:35]\n\t"
        "v_exp_f32 v92, v76\n\t"
        "v_exp_f32 v93, v77\n\t"
        "v_exp_f32 v94, v78\n\t"
        "v_exp_f32 v95, v79\n\t"
        "v_cvt_pk_bf16_f32 v88, v92, v93\n\t"
        "v_cvt_pk_bf16_f32 v89, v94, v95\n\t"
        "v_mfma_f32_32x32x16_bf16 v[36:51], v[100:103], v[84:87], v[36:51]\n\t"
        "v_exp_f32 v92, v80\n\t"
        "v_exp_f32 v93, v81\n\t"
        "v_exp_f32 v94, v82\n\t"
        "v_exp_f32 v95, v83\n\t"
        "v_cvt_pk_bf16_f32 v90, v92, v93\n\t"
        "v_cvt_pk_bf16_f32 v91, v94, v95\n\t"
        "s_waitcnt lgkmcnt(0)\n\t"
        "v_mfma_f32_32x32x16_bf16 v[68:83], v[108:111], v[4:7], 0\n\t"
        "v_exp_f32 v92, v52\n\t"
        "v_exp_f32 v93, v53\n\t"
        "v_exp_f32 v94, v54\n\t"
        "v_exp_f32 v95, v55\n\t"
        "v_cvt_pk_bf16_f32 v84, v92, v93\n\t"
        "v_cvt_pk_bf16_f32 v85, v94, v95\n\t"
        "v_mfma_f32_32x32x8_bf16 v[68:83], v[122:123], v[10:11], v[68:83]\n\t"
        "v_exp_f32 v92, v56\n\t"
        "v_exp_f32 v93, v57\n\t"
        "v_exp_f32 v94, v58\n\t"
        "v_exp_f32 v95, v59\n\t"
        "v_cvt_pk_bf16_f32 v86, v92, v93\n\t"
        "v_cvt_pk_bf16_f32 v87, v94, v95\n\t"
        "v_mfma_f32_32x32x16_bf16 v[36:51], v[104:107], v[88:91], v[36:51]\n\t"
        "v_exp_f32 v92, v60\n\t"
        "v_exp_f32 v93, v61\n\t"
        "v_exp_f32 v94, v62\n\t"
        "v_exp_f32 v95, v63\n\t"
        "v_cvt_pk_bf16_f32 v88, v92, v93\n\t"
        "v_cvt_pk_bf16_f32 v89, v94, v95\n\t"
        "v_mfma_f32_32x32x16_bf16 v[20:35], v[112:115], v[84:87], v[20:35]\n\t"
        "v_exp_f32 v92, v64\n\t"
        "v_exp_f32 v93, v65\n\t"
        "v_exp_f32 v94, v66\n\t"
        "v_exp_f32 v95, v67\n\t"
        "v_cvt_pk_bf16_f32 v90, v92, v93\n\t"
        "v_cvt_pk_bf16_f32 v91, v94, v95\n\t"
        "s_waitcnt vmcnt(0)\n\t"
        "v_mov_b32 v18, %[cn]\n\t"
        "v_mov_b32 v92, 1\n\t"
        "s_mov_b64 exec, 1\n\t"
        "ds_add_u32 v18, v92 offset:4\n\t"
        "s_mov_b64 exec, -1\n\t"
        "ds_read_b128 v[108:111], v12 offset:3072\n\t"
        "ds_read_b64 v[122:123], v13 offset:768\n\t"
        "ds_read_b128 v[100:103], v14 offset:2560\n\t"
        "ds_read_b128 v[104:107], v14 offset:3200\n\t"
        "s_setprio 0\n\t"
        "v_mfma_f32_32x32x16_bf16 v[52:67], v[96:99], v[0:3], 0\n\t"
        "v_exp_f32 v92, v68\n\t"
        "v_exp_f32 v93, v69\n\t"
        "v_exp_f32 v94, v70\n\t"
        "v_exp_f32 v95, v71\n\t"
        "v_cvt_pk_bf16_f32 v84, v92, v93\n\t"
        "v_cvt_pk_bf16_f32 v85, v94, v95\n\t"
        "v_mfma_f32_32x32x8_bf16 v[52:67], v[120:121], v[8:9], v[52:67]\n\t"
        "v_exp_f32 v92, v72\n\t"
        "v_exp_f32 v93, v73\n\t"
        "v_exp_f32 v94, v74\n\t"
        "v_exp_f32 v95, v75\n\t"
        "v_cvt_pk_bf16_f32 v86, v92, v93\n\t"
        "v_cvt_pk_bf16_f32 v87, v94, v95\n\t"
        "v_mfma_f32_32x32x16_bf16 v[20:35], v[116:119], v[88:91], v[20:35]\n\t"
        "v_exp_f32 v92, v76\n\t"
        "v_exp_f32 v93, v77\n\t"
        "v_exp_f32 v94, v78\n\t"
        "v_exp_f32 v95, v79\n\t"
        "v_cvt_pk_bf16_f32 v88, v92, v93\n\t"
        "v_cvt_pk_bf16_f32 v89, v94, v95\n\t"
        "v_mfma_f32_32x32x16_bf16 v[36:51], v[112:115], v[84:87], v[36:51]\n\t"
        "v_exp_f32 v92, v80\n\t"
        "v_exp_f32 v93, v81\n\t"
        "v_exp_f32 v94, v82\n\t"
        "v_exp_f32 v95, v83\n\t"
        "v_cvt_pk_bf16_f32 v90, v92, v93\n\t"
        "v_cvt_pk_bf16_f32 v91, v94, v95\n\t"
        "s_waitcnt lgkmcnt(0)\n\t"
        "v_mov_b32 v18, %[cn]\n\t"
        "ds_read_b32 v18, v18 offset:4\n\t"
        "v_mfma_f32_32x32x16_bf16 v[68:83], v[96:99], v[4:7], 0\n\t"
        "v_exp_f32 v92, v52\n\t"
        "v_exp_f32 v93, v53\n\t"
        "v_exp_f32 v94, v54\n\t"
        "v_exp_f32 v95, v55\n\t"
        "v_cvt_pk_bf16_f32 v84, v92, v93\n\t"
        "v_cvt_pk_bf16_f32 v85, v94, v95\n\t"
        "v_mfma_f32_32x32x8_bf16 v[68:83], v[120:121], v[10:11], v[68:83]\n\t"
        "v_exp_f32 v92, v56\n\t"
        "v_exp_f32 v93, v57\n\t"
        "v_exp_f32 v94, v58\n\t"
        "v_exp_f32 v95, v59\n\t"
        "v_cvt_pk_bf16_f32 v86, v92, v93\n\t"
        "v_cvt_pk_bf16_f32 v87, v94, v95\n\t"
        "v_mfma_f32_32x32x16_bf16 v[36:51], v[116:119], v[88:91], v[36:51]\n\t"
        "v_exp_f32 v92, v60\n\t"
        "v_exp_f32 v93, v61\n\t"
        "v_exp_f32 v94, v62\n\t"
        "v_exp_f32 v95, v63\n\t"
        "v_cvt_pk_bf16_f32 v88, v92, v93\n\t"
        "v_cvt_pk_bf16_f32 v89, v94, v95\n\t"
        "v_mfma_f32_32x32x16_bf16 v[20:35], v[100:103], v[84:87], v[20:35]\n\t"
        "v_exp_f32 v92, v64\n\t"
        "v_exp_f32 v93, v65\n\t"
        "v_exp_f32 v94, v66\n\t"
        "v_exp_f32 v95, v67\n\t"
        "v_cvt_pk_bf16_f32 v90, v92, v93\n\t"
        "v_cvt_pk_bf16_f32 v91, v94, v95\n\t"
        "s_waitcnt lgkmcnt(0)\n\t"
        "v_readfirstlane_b32 %[t], v18\n\t"
        "s_nop 0\n\t"
        "s_cmp_ge_u32 %[t], 8\n\t"
        "s_cbranch_scc1 AOK1_%=\n\t"
        "ASP1_%=:\n\t"
        "s_sleep 1\n\t"
        "v_mov_b32 v18, %[cn]\n\t"
        "ds_read_b32 v18, v18 offset:4\n\t"
        "s_waitcnt lgkmcnt(0)\n\t"
        "v_readfirstlane_b32 %[t], v18\n\t"
        "s_nop 0\n\t"
        "s_cmp_ge_u32 %[t], 8\n\t"
        "s_cbranch_scc0 ASP1_%=\n\t"
        "AOK1_%=:\n\t"
        "ds_read_b128 v[96:99], v12 offset:20480\n\t"
        "ds_read_b64 v[120:121], v13 offset:20480\n\t"
        "ds_read_b128 v[112:115], v14 offset:3840\n\t"
        "ds_read_b128 v[116:119], v14 offset:4480\n\t"
        "s_setprio 3\n\t"
        "v_mfma_f32_32x32x16_bf16 v[52:67], v[108:111], v[0:3], 0\n\t"
        "s_add_u32 m0, %[m0], 40960\n\t"
        "v_exp_f32 v92, v68\n\t"
        "v_exp_f32 v93, v69\n\t"
        "global_load_lds_dwordx4 v15, s[40:41] sc1\n\t"
        "v_exp_f32 v94, v70\n\t"
        "v_exp_f32 v95, v71\n\t"
        "v_cvt_pk_bf16_f32 v84, v92, v93\n\t"
        "v_cvt_pk_bf16_f32 v85, v94, v95\n\t"
        "v_mfma_f32_32x32x8_bf16 v[52:67], v[122:123], v[8:9], v[52:67]\n\t"
        "s_add_u32 m0, %[m1], 40960\n\t"
        "v_exp_f32 v92, v72\n\t"
        "v_exp_f32 v93, v73\n\t"
        "global_load_lds_dwordx4 v16, s[40:41] sc1\n\t"
        "v_exp_f32 v94, v74\n\t"
        "v_exp_f32 v95, v75\n\t"
        "v_cvt_pk_bf16_f32 v86, v92, v93\n\t"
        "v_cvt_pk_bf16_f32 v87, v94, v95\n\t"
        "v_mfma_f32_32x32x16_bf16 v[20:35], v[104:107], v[88:91], v[20:35]\n\t"
        "s_add_u32 m0, %[m2], 40960\n\t"
        "v_exp_f32 v92, v76\n\t"
        "v_exp_f32 v93, v77\n\t"
        "global_load_lds_dwordx4 v17, s[40:41] sc1\n\t"
        "v_exp_f32 v94, v78\n\t"
        "v_exp_f32 v95, v79\n\t"
        "v_cvt_pk_bf16_f32 v88, v92, v93\n\t"
        "v_cvt_pk_bf16_f32 v89, v94, v95\n\t"
        "v_mfma_f32_32x32x16_bf16 v[36:51], v[100:103], v[84:87], v[36:51]\n\t"
        "v_exp_f32 v92, v80\n\t"
        "v_exp_f32 v93, v81\n\t"
        "v_exp_f32 v94, v82\n\t"
        "v_exp_f32 v95, v83\n\t"
        "v_cvt_pk_bf16_f32 v90, v92, v93\n\t"
        "v_cvt_pk_bf16_f32 v91, v94, v95\n\t"
        "s_add_u32 s40, s40, 10240\n\t"
        "s_addc_u32 s41, s41, 0\n\t"
        "s_waitcnt lgkmcnt(0)\n\t"
        "v_mfma_f32_32x32x16_bf16 v[68:83], v[108:111], v[4:7], 0\n\t"
        "v_exp_f32 v92, v52\n\t"
        "v_exp_f32 v93, v53\n\t"
        "v_exp_f32 v94, v54\n\t"
        "v_exp_f32 v95, v55\n\t"
        "v_cvt_pk_bf16_f32 v84, v92, v93\n\t"
        "v_cvt_pk_bf16_f32 v85, v94, v95\n\t"
        "v_mfma_f32_32x32x8_bf16 v[68:83], v[122:123], v[10:11], v[68:83]\n\t"
        "v_exp_f32 v92, v56\n\t"
        "v_exp_f32 v93, v57\n\t"
        "v_exp_f32 v94, v58\n\t"
        "v_exp_f32 v95, v59\n\t"
        "v_cvt_pk_bf16_f32 v86, v92, v93\n\t"
        "v_cvt_pk_bf16_f32 v87, v94, v95\n\t"
        "v_mfma_f32_32x32x16_bf16 v[36:51], v[104:107], v[88:91], v[36:51]\n\t"
        "v_exp_f32 v92, v60\n\t"
        "v_exp_f32 v93, v61\n\t"
        "v_exp_f32 v94, v62\n\t"
        "v_exp_f32 v95, v63\n\t"
        "v_cvt_pk_bf16_f32 v88, v92, v93\n\t"
        "v_cvt_pk_bf16_f32 v89, v94, v95\n\t"
        "v_mfma_f32_32x32x16_bf16 v[20:35], v[112:115], v[84:87], v[20:35]\n\t"
        "v_exp_f32 v92, v64\n\t"
        "v_exp_f32 v93, v65\n\t"
        "v_exp_f32 v94, v66\n\t"
        "v_exp_f32 v95, v67\n\t"
        "v_cvt_pk_bf16_f32 v90, v92, v93\n\t"
        "v_cvt_pk_bf16_f32 v91, v94, v95\n\t"
        "ds_read_b128 v[108:111], v12 offset:21504\n\t"
        "ds_read_b64 v[122:123], v13 offset:20736\n\t"
        "ds_read_b128 v[100:103], v14 offset:20480\n\t"
        "ds_read_b128 v[104:107], v14 offset:21120\n\t"
        "s_setprio 2\n\t"
        "v_mfma_f32_32x32x16_bf16 v[52:67], v[96:99], v[0:3], 0\n\t"
        "v_exp_f32 v92, v68\n\t"
        "v_exp_f32 v93, v69\n\t"
        "v_exp_f32 v94, v70\n\t"
        "v_exp_f32 v95, v71\n\t"
        "v_cvt_pk_bf16_f32 v84, v92, v93\n\t"
        "v_cvt_pk_bf16_f32 v85, v94, v95\n\t"
        "v_mfma_f32_32x32x8_bf16 v[52:67], v[120:121], v[8:9], v[52:67]\n\t"
        "v_exp_f32 v92, v72\n\t"
        "v_exp_f32 v93, v73\n\t"
        "v_exp_f32 v94, v74\n\t"
        "v_exp_f32 v95, v75\n\t"
        "v_cvt_pk_bf16_f32 v86, v92, v93\n\t"
        "v_cvt_pk_bf16_f32 v87, v94, v95\n\t"
        "v_mfma_f32_32x32x16_bf16 v[20:35], v[116:119], v[88:91], v[20:35]\n\t"
        "v_exp_f32 v92, v76\n\t"
        "v_exp_f32 v93, v77\n\t"
        "v_exp_f32 v94, v78\n\t"
        "v_exp_f32 v95, v79\n\t"
        "v_cvt_pk_bf16_f32 v88, v92, v93\n\t"
        "v_cvt_pk_bf16_f32 v89, v94, v95\n\t"
        "v_mfma_f32_32x32x16_bf16 v[36:51], v[112:115], v[84:87], v[36:51]\n\t"
        "v_exp_f32 v92, v80\n\t"
        "v_exp_f32 v93, v81\n\t"
        "v_exp_f32 v94, v82\n\t"
        "v_exp_f32 v95, v83\n\t"
        "v_cvt_pk_bf16_f32 v90, v92, v93\n\t"
        "v_cvt_pk_bf16_f32 v91, v94, v95\n\t"
        "s_waitcnt lgkmcnt(0)\n\t"
        "v_mfma_f32_32x32x16_bf16 v[68:83], v[96:99], v[4:7], 0\n\t"
        "v_exp_f32 v92, v52\n\t"
        "v_exp_f32 v93, v53\n\t"
        "v_exp_f32 v94, v54\n\t"
        "v_exp_f32 v95, v55\n\t"
        "v_cvt_pk_bf16_f32 v84, v92, v93\n\t"
        "v_cvt_pk_bf16_f32 v85, v94, v95\n\t"
        "v_mfma_f32_32x32x8_bf16 v[68:83], v[120:121], v[10:11], v[68:83]\n\t"
        "v_exp_f32 v92, v56\n\t"
        "v_exp_f32 v93, v57\n\t"
        "v_exp_f32 v94, v58\n\t"
        "v_exp_f32 v95, v59\n\t"
        "v_cvt_pk_bf16_f32 v86, v92, v93\n\t"
        "v_cvt_pk_bf16_f32 v87, v94, v95\n\t"
        "v_mfma_f32_32x32x16_bf16 v[36:51], v[116:119], v[88:91], v[36:51]\n\t"
        "v_exp_f32 v92, v60\n\t"
        "v_exp_f32 v93, v61\n\t"
        "v_exp_f32 v94, v62\n\t"
        "v_exp_f32 v95, v63\n\t"
        "v_cvt_pk_bf16_f32 v88, v92, v93\n\t"
        "v_cvt_pk_bf16_f32 v89, v94, v95\n\t"
        "v_mfma_f32_32x32x16_bf16 v[20:35], v[100:103], v[84:87], v[20:35]\n\t"
        "v_exp_f32 v92, v64\n\t"
        "v_exp_f32 v93, v65\n\t"
        "v_exp_f32 v94, v66\n\t"
        "v_exp_f32 v95, v67\n\t"
        "v_cvt_pk_bf16_f32 v90, v92, v93\n\t"
        "v_cvt_pk_bf16_f32 v91, v94, v95\n\t"
        "ds_read_b128 v[96:99], v12 offset:22528\n\t"
        "ds_read_b64 v[120:121], v13 offset:20992\n\t"
        "ds_read_b128 v[112:115], v14 offset:21760\n\t"
        "ds_read_b128 v[116:119], v14 offset:22400\n\t"
        "s_setprio 1\n\t"
        "v_mfma_f32_32x32x16_bf16 v[52:67], v[108:111], v[0:3], 0\n\t"
        "v_exp_f32 v92, v68\n\t"
        "v_exp_f32 v93, v69\n\t"
        "v_exp_f32 v94, v70\n\t"
        "v_exp_f32 v95, v71\n\t"
        "v_cvt_pk_bf16_f32 v84, v92, v93\n\t"
        "v_cvt_pk_bf16_f32 v85, v94, v95\n\t"
        "v_mfma_f32_32x32x8_bf16 v[52:67], v[122:123], v[8:9], v[52:67]\n\t"
        "v_exp_f32 v92, v72\n\t"
        "v_exp_f32 v93, v73\n\t"
        "v_exp_f32 v94, v74\n\t"
        "v_exp_f32 v95, v75\n\t"
        "v_cvt_pk_bf16_f32 v86, v92, v93\n\t"
        "v_cvt_pk_bf16_f32 v87, v94, v95\n\t"
        "v_mfma_f32_32x32x16_bf16 v[20:35], v[104:107], v[88:91], v[20:35]\n\t"
        "v_exp_f32 v92, v76\n\t"
        "v_exp_f32 v93, v77\n\t"
        "v_exp_f32 v94, v78\n\t"
        "v_exp_f32 v95, v79\n\t"
        "v_cvt_pk_bf16_f32 v88, v92, v93\n\t"
        "v_cvt_pk_bf16_f32 v89, v94, v95\n\t"
        "v_mfma_f32_32x32x16_bf16 v[36:51], v[100:103], v[84:87], v[36:51]\n\t"
        "v_exp_f32 v92, v80\n\t"
        "v_exp_f32 v93, v81\n\t"
        "v_exp_f32 v94, v82\n\t"
        "v_exp_f32 v95, v83\n\t"
        "v_cvt_pk_bf16_f32 v90, v92, v93\n\t"
        "v_cvt_pk_bf16_f32 v91, v94, v95\n\t"
        "s_waitcnt lgkmcnt(0)\n\t"
        "v_mfma_f32_32x32x16_bf16 v[68:83], v[108:111], v[4:7], 0\n\t"
        "v_exp_f32 v92, v52\n\t"
        "v_exp_f32 v93, v53\n\t"
        "v_exp_f32 v94, v54\n\t"
        "v_exp_f32 v95, v55\n\t"
        "v_cvt_pk_bf16_f32 v84, v92, v93\n\t"
        "v_cvt_pk_bf16_f32 v85, v94, v95\n\t"
        "v_mfma_f32_32x32x8_bf16 v[68:83], v[122:123], v[10:11], v[68:83]\n\t"
        "v_exp_f32 v92, v56\n\t"
        "v_exp_f32 v93, v57\n\t"
        "v_exp_f32 v94, v58\n\t"
        "v_exp_f32 v95, v59\n\t"
        "v_cvt_pk_bf16_f32 v86, v92, v93\n\t"
        "v_cvt_pk_bf16_f32 v87, v94, v95\n\t"
        "v_mfma_f32_32x32x16_bf16 v[36:51], v[104:107], v[88:91], v[36:51]\n\t"
        "v_exp_f32 v92, v60\n\t"
        "v_exp_f32 v93, v61\n\t"
        "v_exp_f32 v94, v62\n\t"
        "v_exp_f32 v95, v63\n\t"
        "v_cvt_pk_bf16_f32 v88, v92, v93\n\t"
        "v_cvt_pk_bf16_f32 v89, v94, v95\n\t"
        "v_mfma_f32_32x32x16_bf16 v[20:35], v[112:115], v[84:87], v[20:35]\n\t"
        "v_exp_f32 v92, v64\n\t"
        "v_exp_f32 v93, v65\n\t"
        "v_exp_f32 v94, v66\n\t"
        "v_exp_f32 v95, v67\n\t"
        "v_cvt_pk_bf16_f32 v90, v92, v93\n\t"
        "v_cvt_pk_bf16_f32 v91, v94, v95\n\t"
        "s_waitcnt vmcnt(0)\n\t"
        "v_mov_b32 v18, %[cn]\n\t"
        "v_mov_b32 v92, 1\n\t"
        "s_mov_b64 exec, 1\n\t"
        "ds_add_u32 v18, v92 offset:8\n\t"
        "s_mov_b64 exec, -1\n\t"
        "ds_read_b128 v[108:111], v12 offset:23552\n\t"
        "ds_read_b64 v[122:123], v13 offset:21248\n\t"
        "ds_read_b128 v[100:103], v14 offset:23040\n\t"
        "ds_read_b128 v[104:107], v14 offset:23680\n\t"
        "s_setprio 0\n\t"
        "v_mfma_f32_32x32x16_bf16 v[52:67], v[96:99], v[0:3], 0\n\t"
        "v_exp_f32 v92, v68\n\t"
        "v_exp_f32 v93, v69\n\t"
        "v_exp_f32 v94, v70\n\t"
        "v_exp_f32 v95, v71\n\t"
        "v_cvt_pk_bf16_f32 v84, v92, v93\n\t"
        "v_cvt_pk_bf16_f32 v85, v94, v95\n\t"
        "v_mfma_f32_32x32x8_bf16 v[52:67], v[120:121], v[8:9], v[52:67]\n\t"
        "v_exp_f32 v92, v72\n\t"
        "v_exp_f32 v93, v73\n\t"
        "v_exp_f32 v94, v74\n\t"
        "v_exp_f32 v95, v75\n\t"
        "v_cvt_pk_bf16_f32 v86, v92, v93\n\t"
        "v_cvt_pk_bf16_f32 v87, v94, v95\n\t"
        "v_mfma_f32_32x32x16_bf16 v[20:35], v[116:119], v[88:91], v[20:35]\n\t"
        "v_exp_f32 v92, v76\n\t"
        "v_exp_f32 v93, v77\n\t"
        "v_exp_f32 v94, v78\n\t"
        "v_exp_f32 v95, v79\n\t"
        "v_cvt_pk_bf16_f32 v88, v92, v93\n\t"
        "v_cvt_pk_bf16_f32 v89, v94, v95\n\t"
        "v_mfma_f32_32x32x16_bf16 v[36:51], v[112:115], v[84:87], v[36:51]\n\t"
        "v_exp_f32 v92, v80\n\t"
        "v_exp_f32 v93, v81\n\t"
        "v_exp_f32 v94, v82\n\t"
        "v_exp_f32 v95, v83\n\t"
        "v_cvt_pk_bf16_f32 v90, v92, v93\n\t"
        "v_cvt_pk_bf16_f32 v91, v94, v95\n\t"
        "s_waitcnt lgkmcnt(0)\n\t"
        "v_mov_b32 v18, %[cn]\n\t"
        "ds_read_b32 v18, v18 offset:8\n\t"
        "v_mfma_f32_32x32x16_bf16 v[68:83], v[96:99], v[4:7], 0\n\t"
        "v_exp_f32 v92, v52\n\t"
        "v_exp_f32 v93, v53\n\t"
        "v_exp_f32 v94, v54\n\t"
        "v_exp_f32 v95, v55\n\t"
        "v_cvt_pk_bf16_f32 v84, v92, v93\n\t"
        "v_cvt_pk_bf16_f32 v85, v94, v95\n\t"
        "v_mfma_f32_32x32x8_bf16 v[68:83], v[120:121], v[10:11], v[68:83]\n\t"
        "v_exp_f32 v92, v56\n\t"
        "v_exp_f32 v93, v57\n\t"
        "v_exp_f32 v94, v58\n\t"
        "v_exp_f32 v95, v59\n\t"
        "v_cvt_pk_bf16_f32 v86, v92, v93\n\t"
        "v_cvt_pk_bf16_f32 v87, v94, v95\n\t"
        "v_mfma_f32_32x32x16_bf16 v[36:51], v[116:119], v[88:91], v[36:51]\n\t"
        "v_exp_f32 v92, v60\n\t"
        "v_exp_f32 v93, v61\n\t"
        "v_exp_f32 v94, v62\n\t"
        "v_exp_f32 v95, v63\n\t"
        "v_cvt_pk_bf16_f32 v88, v92, v93\n\t"
        "v_cvt_pk_bf16_f32 v89, v94, v95\n\t"
        "v_mfma_f32_32x32x16_bf16 v[20:35], v[100:103], v[84:87], v[20:35]\n\t"
        "v_exp_f32 v92, v64\n\t"
        "v_exp_f32 v93, v65\n\t"
        "v_exp_f32 v94, v66\n\t"
        "v_exp_f32 v95, v67\n\t"
        "v_cvt_pk_bf16_f32 v90, v92, v93\n\t"
        "v_cvt_pk_bf16_f32 v91, v94, v95\n\t"
        "s_waitcnt lgkmcnt(0)\n\t"
        "v_readfirstlane_b32 %[t], v18\n\t"
        "s_nop 0\n\t"
        "s_cmp_ge_u32 %[t], 8\n\t"
        "s_cbranch_scc1 AOK2_%=\n\t"
        "ASP2_%=:\n\t"
        "s_sleep 1\n\t"
        "v_mov_b32 v18, %[cn]\n\t"
        "ds_read_b32 v18, v18 offset:8\n\t"
        "s_waitcnt lgkmcnt(0)\n\t"
        "v_readfirstlane_b32 %[t], v18\n\t"
        "s_nop 0\n\t"
        "s_cmp_ge_u32 %[t], 8\n\t"
        "s_cbranch_scc0 ASP2_%=\n\t"
        "AOK2_%=:\n\t"
        "ds_read_b128 v[96:99], v12 offset:40960\n\t"
        "ds_read_b64 v[120:121], v13 offset:40960\n\t"
        "ds_read_b128 v[112:115], v14 offset:24320\n\t"
        "ds_read_b128 v[116:119], v14 offset:24960\n\t"
        "s_setprio 3\n\t"
        "v_mfma_f32_32x32x16_bf16 v[52:67], v[108:111], v[0:3], 0\n\t"
        "s_add_u32 m0, %[m0], 0\n\t"
        "v_exp_f32 v92, v68\n\t"
        "v_exp_f32 v93, v69\n\t"
        "global_load_lds_dwordx4 v15, s[40:41] sc1\n\t"
        "v_exp_f32 v94, v70\n\t"
        "v_exp_f32 v95, v71\n\t"
        "v_cvt_pk_bf16_f32 v84, v92, v93\n\t"
        "v_cvt_pk_bf16_f32 v85, v94, v95\n\t"
        "v_mfma_f32_32x32x8_bf16 v[52:67], v[122:123], v[8:9], v[52:67]\n\t"
        "s_add_u32 m0, %[m1], 0\n\t"
        "v_exp_f32 v92, v72\n\t"
        "v_exp_f32 v93, v73\n\t"
        "global_load_lds_dwordx4 v16, s[40:41] sc1\n\t"
        "v_exp_f32 v94, v74\n\t"
        "v_exp_f32 v95, v75\n\t"
        "v_cvt_pk_bf16_f32 v86, v92, v93\n\t"
        "v_cvt_pk_bf16_f32 v87, v94, v95\n\t"
        "v_mfma_f32_32x32x16_bf16 v[20:35], v[104:107], v[88:91], v[20:35]\n\t"
        "s_add_u32 m0, %[m2], 0\n\t"
        "v_exp_f32 v92, v76\n\t"
        "v_exp_f32 v93, v77\n\t"
        "global_load_lds_dwordx4 v17, s[40:41] sc1\n\t"
        "v_exp_f32 v94, v78\n\t"
        "v_exp_f32 v95, v79\n\t"
        "v_cvt_pk_bf16_f32 v88, v92, v93\n\t"
        "v_cvt_pk_bf16_f32 v89, v94, v95\n\t"
        "v_mfma_f32_32x32x16_bf16 v[36:51], v[100:103], v[84:87], v[36:51]\n\t"
        "v_exp_f32 v92, v80\n\t"
        "v_exp_f32 v93, v81\n\t"
        "v_exp_f32 v94, v82\n\t"
        "v_exp_f32 v95, v83\n\t"
        "v_cvt_pk_bf16_f32 v90, v92, v93\n\t"
        "v_cvt_pk_bf16_f32 v91, v94, v95\n\t"
        "s_add_u32 s40, s40, 10240\n\t"
        "s_addc_u32 s41, s41, 0\n\t"
        "s_waitcnt lgkmcnt(0)\n\t"
        "v_mfma_f32_32x32x16_bf16 v[68:83], v[108:111], v[4:7], 0\n\t"
        "v_exp_f32 v92, v52\n\t"
        "v_exp_f32 v93, v53\n\t"
        "v_exp_f32 v94, v54\n\t"
        "v_exp_f32 v95, v55\n\t"
        "v_cvt_pk_bf16_f32 v84, v92, v93\n\t"
        "v_cvt_pk_bf16_f32 v85, v94, v95\n\t"
        "v_mfma_f32_32x32x8_bf16 v[68:83], v[122:123], v[10:11], v[68:83]\n\t"
        "v_exp_f32 v92, v56\n\t"
        "v_exp_f32 v93, v57\n\t"
        "v_exp_f32 v94, v58\n\t"
        "v_exp_f32 v95, v59\n\t"
        "v_cvt_pk_bf16_f32 v86, v92, v93\n\t"
        "v_cvt_pk_bf16_f32 v87, v94, v95\n\t"
        "v_mfma_f32_32x32x16_bf16 v[36:51], v[104:107], v[88:91], v[36:51]\n\t"
        "v_exp_f32 v92, v60\n\t"
        "v_exp_f32 v93, v61\n\t"
        "v_exp_f32 v94, v62\n\t"
        "v_exp_f32 v95, v63\n\t"
        "v_cvt_pk_bf16_f32 v88, v92, v93\n\t"
        "v_cvt_pk_bf16_f32 v89, v94, v95\n\t"
        "v_mfma_f32_32x32x16_bf16 v[20:35], v[112:115], v[84:87], v[20:35]\n\t"
        "v_exp_f32 v92, v64\n\t"
        "v_exp_f32 v93, v65\n\t"
        "v_exp_f32 v94, v66\n\t"
        "v_exp_f32 v95, v67\n\t"
        "v_cvt_pk_bf16_f32 v90, v92, v93\n\t"
        "v_cvt_pk_bf16_f32 v91, v94, v95\n\t"
        "ds_read_b128 v[108:111], v12 offset:41984\n\t"
        "ds_read_b64 v[122:123], v13 offset:41216\n\t"
        "ds_read_b128 v[100:103], v14 offset:40960\n\t"
        "ds_read_b128 v[104:107], v14 offset:41600\n\t"
        "s_setprio 2\n\t"
        "v_mfma_f32_32x32x16_bf16 v[52:67], v[96:99], v[0:3], 0\n\t"
        "v_exp_f32 v92, v68\n\t"
        "v_exp_f32 v93, v69\n\t"
        "v_exp_f32 v94, v70\n\t"
        "v_exp_f32 v95, v71\n\t"
        "v_cvt_pk_bf16_f32 v84, v92, v93\n\t"
        "v_cvt_pk_bf16_f32 v85, v94, v95\n\t"
        "v_mfma_f32_32x32x8_bf16 v[52:67], v[120:121], v[8:9], v[52:67]\n\t"
        "v_exp_f32 v92, v72\n\t"
        "v_exp_f32 v93, v73\n\t"
        "v_exp_f32 v94, v74\n\t"
        "v_exp_f32 v95, v75\n\t"
        "v_cvt_pk_bf16_f32 v86, v92, v93\n\t"
        "v_cvt_pk_bf16_f32 v87, v94, v95\n\t"
        "v_mfma_f32_32x32x16_bf16 v[20:35], v[116:119], v[88:91], v[20:35]\n\t"
        "v_exp_f32 v92, v76\n\t"
        "v_exp_f32 v93, v77\n\t"
        "v_exp_f32 v94, v78\n\t"
        "v_exp_f32 v95, v79\n\t"
        "v_cvt_pk_bf16_f32 v88, v92, v93\n\t"
        "v_cvt_pk_bf16_f32 v89, v94, v95\n\t"
        "v_mfma_f32_32x32x16_bf16 v[36:51], v[112:115], v[84:87], v[36:51]\n\t"
        "v_exp_f32 v92, v80\n\t"
        "v_exp_f32 v93, v81\n\t"
        "v_exp_f32 v94, v82\n\t"
        "v_exp_f32 v95, v83\n\t"
        "v_cvt_pk_bf16_f32 v90, v92, v93\n\t"
        "v_cvt_pk_bf16_f32 v91, v94, v95\n\t"
        "s_waitcnt lgkmcnt(0)\n\t"
        "v_mfma_f32_32x32x16_bf16 v[68:83], v[96:99], v[4:7], 0\n\t"
        "v_exp_f32 v92, v52\n\t"
        "v_exp_f32 v93, v53\n\t"
        "v_exp_f32 v94, v54\n\t"
        "v_exp_f32 v95, v55\n\t"
        "v_cvt_pk_bf16_f32 v84, v92, v93\n\t"
        "v_cvt_pk_bf16_f32 v85, v94, v95\n\t"
        "v_mfma_f32_32x32x8_bf16 v[68:83], v[120:121], v[10:11], v[68:83]\n\t"
        "v_exp_f32 v92, v56\n\t"
        "v_exp_f32 v93, v57\n\t"
        "v_exp_f32 v94, v58\n\t"
        "v_exp_f32 v95, v59\n\t"
        "v_cvt_pk_bf16_f32 v86, v92, v93\n\t"
        "v_cvt_pk_bf16_f32 v87, v94, v95\n\t"
        "v_mfma_f32_32x32x16_bf16 v[36:51], v[116:119], v[88:91], v[36:51]\n\t"
        "v_exp_f32 v92, v60\n\t"
        "v_exp_f32 v93, v61\n\t"
        "v_exp_f32 v94, v62\n\t"
        "v_exp_f32 v95, v63\n\t"
        "v_cvt_pk_bf16_f32 v88, v92, v93\n\t"
        "v_cvt_pk_bf16_f32 v89, v94, v95\n\t"
        "v_mfma_f32_32x32x16_bf16 v[20:35], v[100:103], v[84:87], v[20:35]\n\t"
        "v_exp_f32 v92, v64\n\t"
        "v_exp_f32 v93, v65\n\t"
        "v_exp_f32 v94, v66\n\t"
        "v_exp_f32 v95, v67\n\t"
        "v_cvt_pk_bf16_f32 v90, v92, v93\n\t"
        "v_cvt_pk_bf16_f32 v91, v94, v95\n\t"
        "ds_read_b128 v[96:99], v12 offset:43008\n\t"
        "ds_read_b64 v[120:121], v13 offset:41472\n\t"
        "ds_read_b128 v[112:115], v14 offset:42240\n\t"
        "ds_read_b128 v[116:119], v14 offset:42880\n\t"
        "s_setprio 1\n\t"
        "v_mfma_f32_32x32x16_bf16 v[52:67], v[108:111], v[0:3], 0\n\t"
        "v_exp_f32 v92, v68\n\t"
        "v_exp_f32 v93, v69\n\t"
        "v_exp_f32 v94, v70\n\t"
        "v_exp_f32 v95, v71\n\t"
        "v_cvt_pk_bf16_f32 v84, v92, v93\n\t"
        "v_cvt_pk_bf16_f32 v85, v94, v95\n\t"
        "v_mfma_f32_32x32x8_bf16 v[52:67], v[122:123], v[8:9], v[52:67]\n\t"
        "v_exp_f32 v92, v72\n\t"
        "v_exp_f32 v93, v73\n\t"
        "v_exp_f32 v94, v74\n\t"
        "v_exp_f32 v95, v75\n\t"
        "v_cvt_pk_bf16_f32 v86, v92, v93\n\t"
        "v_cvt_pk_bf16_f32 v87, v94, v95\n\t"
        "v_mfma_f32_32x32x16_bf16 v[20:35], v[104:107], v[88:91], v[20:35]\n\t"
        "v_exp_f32 v92, v76\n\t"
        "v_exp_f32 v93, v77\n\t"
        "v_exp_f32 v94, v78\n\t"
        "v_exp_f32 v95, v79\n\t"
        "v_cvt_pk_bf16_f32 v88, v92, v93\n\t"
        "v_cvt_pk_bf16_f32 v89, v94, v95\n\t"
        "v_mfma_f32_32x32x16_bf16 v[36:51], v[100:103], v[84:87], v[36:51]\n\t"
        "v_exp_f32 v92, v80\n\t"
        "v_exp_f32 v93, v81\n\t"
        "v_exp_f32 v94, v82\n\t"
        "v_exp_f32 v95, v83\n\t"
        "v_cvt_pk_bf16_f32 v90, v92, v93\n\t"
        "v_cvt_pk_bf16_f32 v91, v94, v95\n\t"
        "s_waitcnt lgkmcnt(0)\n\t"
        "v_mfma_f32_32x32x16_bf16 v[68:83], v[108:111], v[4:7], 0\n\t"
        "v_exp_f32 v92, v52\n\t"
        "v_exp_f32 v93, v53\n\t"
        "v_exp_f32 v94, v54\n\t"
        "v_exp_f32 v95, v55\n\t"
        "v_cvt_pk_bf16_f32 v84, v92, v93\n\t"
        "v_cvt_pk_bf16_f32 v85, v94, v95\n\t"
        "v_mfma_f32_32x32x8_bf16 v[68:83], v[122:123], v[10:11], v[68:83]\n\t"
        "v_exp_f32 v92, v56\n\t"
        "v_exp_f32 v93, v57\n\t"
        "v_exp_f32 v94, v58\n\t"
        "v_exp_f32 v95, v59\n\t"
        "v_cvt_pk_bf16_f32 v86, v92, v93\n\t"
        "v_cvt_pk_bf16_f32 v87, v94, v95\n\t"
        "v_mfma_f32_32x32x16_bf16 v[36:51], v[104:107], v[88:91], v[36:51]\n\t"
        "v_exp_f32 v92, v60\n\t"
        "v_exp_f32 v93, v61\n\t"
        "v_exp_f32 v94, v62\n\t"
        "v_exp_f32 v95, v63\n\t"
        "v_cvt_pk_bf16_f32 v88, v92, v93\n\t"
        "v_cvt_pk_bf16_f32 v89, v94, v95\n\t"
        "v_mfma_f32_32x32x16_bf16 v[20:35], v[112:115], v[84:87], v[20:35]\n\t"
        "v_exp_f32 v92, v64\n\t"
        "v_exp_f32 v93, v65\n\t"
        "v_exp_f32 v94, v66\n\t"
        "v_exp_f32 v95, v67\n\t"
        "v_cvt_pk_bf16_f32 v90, v92, v93\n\t"
        "v_cvt_pk_bf16_f32 v91, v94, v95\n\t"
        "s_waitcnt vmcnt(0)\n\t"
        "v_mov_b32 v18, %[cn]\n\t"
        "v_mov_b32 v92, 1\n\t"
        "s_mov_b64 exec, 1\n\t"
        "ds_add_u32 v18, v92 offset:12\n\t"
        "s_mov_b64 exec, -1\n\t"
        "ds_read_b128 v[108:111], v12 offset:44032\n\t"
        "ds_read_b64 v[122:123], v13 offset:41728\n\t"
        "ds_read_b128 v[100:103], v14 offset:43520\n\t"
        "ds_read_b128 v[104:107], v14 offset:44160\n\t"
        "s_setprio 0\n\t"
        "v_mfma_f32_32x32x16_bf16 v[52:67], v[96:99], v[0:3], 0\n\t"
        "v_exp_f32 v92, v68\n\t"
        "v_exp_f32 v93, v69\n\t"
        "v_exp_f32 v94, v70\n\t"
        "v_exp_f32 v95, v71\n\t"
        "v_cvt_pk_bf16_f32 v84, v92, v93\n\t"
        "v_cvt_pk_bf16_f32 v85, v94, v95\n\t"
        "v_mfma_f32_32x32x8_bf16 v[52:67], v[120:121], v[8:9], v[52:67]\n\t"
        "v_exp_f32 v92, v72\n\t"
        "v_exp_f32 v93, v73\n\t"
        "v_exp_f32 v94, v74\n\t"
        "v_exp_f32 v95, v75\n\t"
        "v_cvt_pk_bf16_f32 v86, v92, v93\n\t"
        "v_cvt_pk_bf16_f32 v87, v94, v95\n\t"
        "v_mfma_f32_32x32x16_bf16 v[20:35], v[116:119], v[88:91], v[20:35]\n\t"
        "v_exp_f32 v92, v76\n\t"
        "v_exp_f32 v93, v77\n\t"
        "v_exp_f32 v94, v78\n\t"
        "v_exp_f32 v95, v79\n\t"
        "v_cvt_pk_bf16_f32 v88, v92, v93\n\t"
        "v_cvt_pk_bf16_f32 v89, v94, v95\n\t"
        "v_mfma_f32_32x32x16_bf16 v[36:51], v[112:115], v[84:87], v[36:51]\n\t"
        "v_exp_f32 v92, v80\n\t"
        "v_exp_f32 v93, v81\n\t"
        "v_exp_f32 v94, v82\n\t"
        "v_exp_f32 v95, v83\n\t"
        "v_cvt_pk_bf16_f32 v90, v92, v93\n\t"
        "v_cvt_pk_bf16_f32 v91, v94, v95\n\t"
        "s_waitcnt lgkmcnt(0)\n\t"
        "v_mov_b32 v18, %[cn]\n\t"
        "ds_read_b32 v18, v18 offset:12\n\t"
        "v_mfma_f32_32x32x16_bf16 v[68:83], v[96:99], v[4:7], 0\n\t"
        "v_exp_f32 v92, v52\n\t"
        "v_exp_f32 v93, v53\n\t"
        "v_exp_f32 v94, v54\n\t"
        "v_exp_f32 v95, v55\n\t"
        "v_cvt_pk_bf16_f32 v84, v92, v93\n\t"
        "v_cvt_pk_bf16_f32 v85, v94, v95\n\t"
        "v_mfma_f32_32x32x8_bf16 v[68:83], v[120:121], v[10:11], v[68:83]\n\t"
        "v_exp_f32 v92, v56\n\t"
        "v_exp_f32 v93, v57\n\t"
        "v_exp_f32 v94, v58\n\t"
        "v_exp_f32 v95, v59\n\t"
        "v_cvt_pk_bf16_f32 v86, v92, v93\n\t"
        "v_cvt_pk_bf16_f32 v87, v94, v95\n\t"
        "v_mfma_f32_32x32x16_bf16 v[36:51], v[116:119], v[88:91], v[36:51]\n\t"
        "v_exp_f32 v92, v60\n\t"
        "v_exp_f32 v93, v61\n\t"
        "v_exp_f32 v94, v62\n\t"
        "v_exp_f32 v95, v63\n\t"
        "v_cvt_pk_bf16_f32 v88, v92, v93\n\t"
        "v_cvt_pk_bf16_f32 v89, v94, v95\n\t"
        "v_mfma_f32_32x32x16_bf16 v[20:35], v[100:103], v[84:87], v[20:35]\n\t"
        "v_exp_f32 v92, v64\n\t"
        "v_exp_f32 v93, v65\n\t"
        "v_exp_f32 v94, v66\n\t"
        "v_exp_f32 v95, v67\n\t"
        "v_cvt_pk_bf16_f32 v90, v92, v93\n\t"
        "v_cvt_pk_bf16_f32 v91, v94, v95\n\t"
        "s_waitcnt lgkmcnt(0)\n\t"
        "v_readfirstlane_b32 %[t], v18\n\t"
        "s_nop 0\n\t"
        "s_cmp_ge_u32 %[t], 8\n\t"
        "s_cbranch_scc1 AOK3_%=\n\t"
        "ASP3_%=:\n\t"
        "s_sleep 1\n\t"
        "v_mov_b32 v18, %[cn]\n\t"
        "ds_read_b32 v18, v18 offset:12\n\t"
        "s_waitcnt lgkmcnt(0)\n\t"
        "v_readfirstlane_b32 %[t], v18\n\t"
        "s_nop 0\n\t"
        "s_cmp_ge_u32 %[t], 8\n\t"
        "s_cbranch_scc0 ASP3_%=\n\t"
        "AOK3_%=:\n\t"
        "ds_read_b128 v[96:99], v12 offset:0\n\t"
        "ds_read_b64 v[120:121], v13 offset:0\n\t"
        "ds_read_b128 v[112:115], v14 offset:44800\n\t"
        "ds_read_b128 v[116:119], v14 offset:45440\n\t"
        "s_setprio 3\n\t"
        "v_mfma_f32_32x32x16_bf16 v[52:67], v[108:111], v[0:3], 0\n\t"
        "s_add_u32 m0, %[m0], 20480\n\t"
        "v_exp_f32 v92, v68\n\t"
        "v_exp_f32 v93, v69\n\t"
        "global_load_lds_dwordx4 v15, s[40:41] sc1\n\t"
        "v_exp_f32 v94, v70\n\t"
        "v_exp_f32 v95, v71\n\t"
        "v_cvt_pk_bf16_f32 v84, v92, v93\n\t"
        "v_cvt_pk_bf16_f32 v85, v94, v95\n\t"
        "v_mfma_f32_32x32x8_bf16 v[52:67], v[122:123], v[8:9], v[52:67]\n\t"
        "s_add_u32 m0, %[m1], 20480\n\t"
        "v_exp_f32 v92, v72\n\t"
        "v_exp_f32 v93, v73\n\t"
        "global_load_lds_dwordx4 v16, s[40:41] sc1\n\t"
        "v_exp_f32 v94, v74\n\t"
        "v_exp_f32 v95, v75\n\t"
        "v_cvt_pk_bf16_f32 v86, v92, v93\n\t"
        "v_cvt_pk_bf16_f32 v87, v94, v95\n\t"
        "v_mfma_f32_32x32x16_bf16 v[20:35], v[104:107], v[88:91], v[20:35]\n\t"
        "s_add_u32 m0, %[m2], 20480\n\t"
        "v_exp_f32 v92, v76\n\t"
        "v_exp_f32 v93, v77\n\t"
        "global_load_lds_dwordx4 v17, s[40:41] sc1\n\t"
        "v_exp_f32 v94, v78\n\t"
        "v_exp_f32 v95, v79\n\t"
        "v_cvt_pk_bf16_f32 v88, v92, v93\n\t"
        "v_cvt_pk_bf16_f32 v89, v94, v95\n\t"
        "v_mfma_f32_32x32x16_bf16 v[36:51], v[100:103], v[84:87], v[36:51]\n\t"
        "v_exp_f32 v92, v80\n\t"
        "v_exp_f32 v93, v81\n\t"
        "v_exp_f32 v94, v82\n\t"
        "v_exp_f32 v95, v83\n\t"
        "v_cvt_pk_bf16_f32 v90, v92, v93\n\t"
        "v_cvt_pk_bf16_f32 v91, v94, v95\n\t"
        "s_add_u32 s40, s40, 10240\n\t"
        "s_addc_u32 s41, s41, 0\n\t"
        "s_waitcnt lgkmcnt(0)\n\t"
        "v_mfma_f32_32x32x16_bf16 v[68:83], v[108:111], v[4:7], 0\n\t"
        "v_exp_f32 v92, v52\n\t"
        "v_exp_f32 v93, v53\n\t"
        "v_exp_f32 v94, v54\n\t"
        "v_exp_f32 v95, v55\n\t"
        "v_cvt_pk_bf16_f32 v84, v92, v93\n\t"
        "v_cvt_pk_bf16_f32 v85, v94, v95\n\t"
        "v_mfma_f32_32x32x8_bf16 v[68:83], v[122:123], v[10:11], v[68:83]\n\t"
        "v_exp_f32 v92, v56\n\t"
        "v_exp_f32 v93, v57\n\t"
        "v_exp_f32 v94, v58\n\t"
        "v_exp_f32 v95, v59\n\t"
        "v_cvt_pk_bf16_f32 v86, v92, v93\n\t"
        "v_cvt_pk_bf16_f32 v87, v94, v95\n\t"
        "v_mfma_f32_32x32x16_bf16 v[36:51], v[104:107], v[88:91], v[36:51]\n\t"
        "v_exp_f32 v92, v60\n\t"
        "v_exp_f32 v93, v61\n\t"
        "v_exp_f32 v94, v62\n\t"
        "v_exp_f32 v95, v63\n\t"
        "v_cvt_pk_bf16_f32 v88, v92, v93\n\t"
        "v_cvt_pk_bf16_f32 v89, v94, v95\n\t"
        "v_mfma_f32_32x32x16_bf16 v[20:35], v[112:115], v[84:87], v[20:35]\n\t"
        "v_exp_f32 v92, v64\n\t"
        "v_exp_f32 v93, v65\n\t"
        "v_exp_f32 v94, v66\n\t"
        "v_exp_f32 v95, v67\n\t"
        "v_cvt_pk_bf16_f32 v90, v92, v93\n\t"
        "v_cvt_pk_bf16_f32 v91, v94, v95\n\t"
        "ds_read_b128 v[108:111], v12 offset:1024\n\t"
        "ds_read_b64 v[122:123], v13 offset:256\n\t"
        "ds_read_b128 v[100:103], v14 offset:0\n\t"
        "ds_read_b128 v[104:107], v14 offset:640\n\t"
        "s_setprio 2\n\t"
        "v_mfma_f32_32x32x16_bf16 v[52:67], v[96:99], v[0:3], 0\n\t"
        "v_exp_f32 v92, v68\n\t"
        "v_exp_f32 v93, v69\n\t"
        "v_exp_f32 v94, v70\n\t"
        "v_exp_f32 v95, v71\n\t"
        "v_cvt_pk_bf16_f32 v84, v92, v93\n\t"
        "v_cvt_pk_bf16_f32 v85, v94, v95\n\t"
        "v_mfma_f32_32x32x8_bf16 v[52:67], v[120:121], v[8:9], v[52:67]\n\t"
        "v_exp_f32 v92, v72\n\t"
        "v_exp_f32 v93, v73\n\t"
        "v_exp_f32 v94, v74\n\t"
        "v_exp_f32 v95, v75\n\t"
        "v_cvt_pk_bf16_f32 v86, v92, v93\n\t"
        "v_cvt_pk_bf16_f32 v87, v94, v95\n\t"
        "v_mfma_f32_32x32x16_bf16 v[20:35], v[116:119], v[88:91], v[20:35]\n\t"
        "v_exp_f32 v92, v76\n\t"
        "v_exp_f32 v93, v77\n\t"
        "v_exp_f32 v94, v78\n\t"
        "v_exp_f32 v95, v79\n\t"
        "v_cvt_pk_bf16_f32 v88, v92, v93\n\t"
        "v_cvt_pk_bf16_f32 v89, v94, v95\n\t"
        "v_mfma_f32_32x32x16_bf16 v[36:51], v[112:115], v[84:87], v[36:51]\n\t"
        "v_exp_f32 v92, v80\n\t"
        "v_exp_f32 v93, v81\n\t"
        "v_exp_f32 v94, v82\n\t"
        "v_exp_f32 v95, v83\n\t"
        "v_cvt_pk_bf16_f32 v90, v92, v93\n\t"
        "v_cvt_pk_bf16_f32 v91, v94, v95\n\t"
        "s_waitcnt lgkmcnt(0)\n\t"
        "v_mfma_f32_32x32x16_bf16 v[68:83], v[96:99], v[4:7], 0\n\t"
        "v_exp_f32 v92, v52\n\t"
        "v_exp_f32 v93, v53\n\t"
        "v_exp_f32 v94, v54\n\t"
        "v_exp_f32 v95, v55\n\t"
        "v_cvt_pk_bf16_f32 v84, v92, v93\n\t"
        "v_cvt_pk_bf16_f32 v85, v94, v95\n\t"
        "v_mfma_f32_32x32x8_bf16 v[68:83], v[120:121], v[10:11], v[68:83]\n\t"
        "v_exp_f32 v92, v56\n\t"
        "v_exp_f32 v93, v57\n\t"
        "v_exp_f32 v94, v58\n\t"
        "v_exp_f32 v95, v59\n\t"
        "v_cvt_pk_bf16_f32 v86, v92, v93\n\t"
        "v_cvt_pk_bf16_f32 v87, v94, v95\n\t"
        "v_mfma_f32_32x32x16_bf16 v[36:51], v[116:119], v[88:91], v[36:51]\n\t"
        "v_exp_f32 v92, v60\n\t"
        "v_exp_f32 v93, v61\n\t"
        "v_exp_f32 v94, v62\n\t"
        "v_exp_f32 v95, v63\n\t"
        "v_cvt_pk_bf16_f32 v88, v92, v93\n\t"
        "v_cvt_pk_bf16_f32 v89, v94, v95\n\t"
        "v_mfma_f32_32x32x16_bf16 v[20:35], v[100:103], v[84:87], v[20:35]\n\t"
        "v_exp_f32 v92, v64\n\t"
        "v_exp_f32 v93, v65\n\t"
        "v_exp_f32 v94, v66\n\t"
        "v_exp_f32 v95, v67\n\t"
        "v_cvt_pk_bf16_f32 v90, v92, v93\n\t"
        "v_cvt_pk_bf16_f32 v91, v94, v95\n\t"
        "ds_read_b128 v[96:99], v12 offset:2048\n\t"
        "ds_read_b64 v[120:121], v13 offset:512\n\t"
        "ds_read_b128 v[112:115], v14 offset:1280\n\t"
        "ds_read_b128 v[116:119], v14 offset:1920\n\t"
        "s_setprio 1\n\t"
        "v_mfma_f32_32x32x16_bf16 v[52:67], v[108:111], v[0:3], 0\n\t"
        "v_exp_f32 v92, v68\n\t"
        "v_exp_f32 v93, v69\n\t"
        "v_exp_f32 v94, v70\n\t"
        "v_exp_f32 v95, v71\n\t"
        "v_cvt_pk_bf16_f32 v84, v92, v93\n\t"
        "v_cvt_pk_bf16_f32 v85, v94, v95\n\t"
        "v_mfma_f32_32x32x8_bf16 v[52:67], v[122:123], v[8:9], v[52:67]\n\t"
        "v_exp_f32 v92, v72\n\t"
        "v_exp_f32 v93, v73\n\t"
        "v_exp_f32 v94, v74\n\t"
        "v_exp_f32 v95, v75\n\t"
        "v_cvt_pk_bf16_f32 v86, v92, v93\n\t"
        "v_cvt_pk_bf16_f32 v87, v94, v95\n\t"
        "v_mfma_f32_32x32x16_bf16 v[20:35], v[104:107], v[88:91], v[20:35]\n\t"
        "v_exp_f32 v92, v76\n\t"
        "v_exp_f32 v93, v77\n\t"
        "v_exp_f32 v94, v78\n\t"
        "v_exp_f32 v95, v79\n\t"
        "v_cvt_pk_bf16_f32 v88, v92, v93\n\t"
        "v_cvt_pk_bf16_f32 v89, v94, v95\n\t"
        "v_mfma_f32_32x32x16_bf16 v[36:51], v[100:103], v[84:87], v[36:51]\n\t"
        "v_exp_f32 v92, v80\n\t"
        "v_exp_f32 v93, v81\n\t"
        "v_exp_f32 v94, v82\n\t"
        "v_exp_f32 v95, v83\n\t"
        "v_cvt_pk_bf16_f32 v90, v92, v93\n\t"
        "v_cvt_pk_bf16_f32 v91, v94, v95\n\t"
        "s_waitcnt lgkmcnt(0)\n\t"
        "v_mfma_f32_32x32x16_bf16 v[68:83], v[108:111], v[4:7], 0\n\t"
        "v_exp_f32 v92, v52\n\t"
        "v_exp_f32 v93, v53\n\t"
        "v_exp_f32 v94, v54\n\t"
        "v_exp_f32 v95, v55\n\t"
        "v_cvt_pk_bf16_f32 v84, v92, v93\n\t"
        "v_cvt_pk_bf16_f32 v85, v94, v95\n\t"
        "v_mfma_f32_32x32x8_bf16 v[68:83], v[122:123], v[10:11], v[68:83]\n\t"
        "v_exp_f32 v92, v56\n\t"
        "v_exp_f32 v93, v57\n\t"
        "v_exp_f32 v94, v58\n\t"
        "v_exp_f32 v95, v59\n\t"
        "v_cvt_pk_bf16_f32 v86, v92, v93\n\t"
        "v_cvt_pk_bf16_f32 v87, v94, v95\n\t"
        "v_mfma_f32_32x32x16_bf16 v[36:51], v[104:107], v[88:91], v[36:51]\n\t"
        "v_exp_f32 v92, v60\n\t"
        "v_exp_f32 v93, v61\n\t"
        "v_exp_f32 v94, v62\n\t"
        "v_exp_f32 v95, v63\n\t"
        "v_cvt_pk_bf16_f32 v88, v92, v93\n\t"
        "v_cvt_pk_bf16_f32 v89, v94, v95\n\t"
        "v_mfma_f32_32x32x16_bf16 v[20:35], v[112:115], v[84:87], v[20:35]\n\t"
        "v_exp_f32 v92, v64\n\t"
        "v_exp_f32 v93, v65\n\t"
        "v_exp_f32 v94, v66\n\t"
        "v_exp_f32 v95, v67\n\t"
        "v_cvt_pk_bf16_f32 v90, v92, v93\n\t"
        "v_cvt_pk_bf16_f32 v91, v94, v95\n\t"
        "s_waitcnt vmcnt(0)\n\t"
        "v_mov_b32 v18, %[cn]\n\t"
        "v_mov_b32 v92, 1\n\t"
        "s_mov_b64 exec, 1\n\t"
        "ds_add_u32 v18, v92 offset:16\n\t"
        "s_mov_b64 exec, -1\n\t"
        "ds_read_b128 v[108:111], v12 offset:3072\n\t"
        "ds_read_b64 v[122:123], v13 offset:768\n\t"
        "ds_read_b128 v[100:103], v14 offset:2560\n\t"
        "ds_read_b128 v[104:107], v14 offset:3200\n\t"
        "s_setprio 0\n\t"
        "v_mfma_f32_32x32x16_bf16 v[52:67], v[96:99], v[0:3], 0\n\t"
        "v_exp_f32 v92, v68\n\t"
        "v_exp_f32 v93, v69\n\t"
        "v_exp_f32 v94, v70\n\t"
        "v_exp_f32 v95, v71\n\t"
        "v_cvt_pk_bf16_f32 v84, v92, v93\n\t"
        "v_cvt_pk_bf16_f32 v85, v94, v95\n\t"
        "v_mfma_f32_32x32x8_bf16 v[52:67], v[120:121], v[8:9], v[52:67]\n\t"
        "v_exp_f32 v92, v72\n\t"
        "v_exp_f32 v93, v73\n\t"
        "v_exp_f32 v94, v74\n\t"
        "v_exp_f32 v95, v75\n\t"
        "v_cvt_pk_bf16_f32 v86, v92, v93\n\t"
        "v_cvt_pk_bf16_f32 v87, v94, v95\n\t"
        "v_mfma_f32_32x32x16_bf16 v[20:35], v[116:119], v[88:91], v[20:35]\n\t"
        "v_exp_f32 v92, v76\n\t"
        "v_exp_f32 v93, v77\n\t"
        "v_exp_f32 v94, v78\n\t"
        "v_exp_f32 v95, v79\n\t"
        "v_cvt_pk_bf16_f32 v88, v92, v93\n\t"
        "v_cvt_pk_bf16_f32 v89, v94, v95\n\t"
        "v_mfma_f32_32x32x16_bf16 v[36:51], v[112:115], v[84:87], v[36:51]\n\t"
        "v_exp_f32 v92, v80\n\t"
        "v_exp_f32 v93, v81\n\t"
        "v_exp_f32 v94, v82\n\t"
        "v_exp_f32 v95, v83\n\t"
        "v_cvt_pk_bf16_f32 v90, v92, v93\n\t"
        "v_cvt_pk_bf16_f32 v91, v94, v95\n\t"
        "s_waitcnt lgkmcnt(0)\n\t"
        "v_mov_b32 v18, %[cn]\n\t"
        "ds_read_b32 v18, v18 offset:16\n\t"
        "v_mfma_f32_32x32x16_bf16 v[68:83], v[96:99], v[4:7], 0\n\t"
        "v_exp_f32 v92, v52\n\t"
        "v_exp_f32 v93, v53\n\t"
        "v_exp_f32 v94, v54\n\t"
        "v_exp_f32 v95, v55\n\t"
        "v_cvt_pk_bf16_f32 v84, v92, v93\n\t"
        "v_cvt_pk_bf16_f32 v85, v94, v95\n\t"
        "v_mfma_f32_32x32x8_bf16 v[68:83], v[120:121], v[10:11], v[68:83]\n\t"
        "v_exp_f32 v92, v56\n\t"
        "v_exp_f32 v93, v57\n\t"
        "v_exp_f32 v94, v58\n\t"
        "v_exp_f32 v95, v59\n\t"
        "v_cvt_pk_bf16_f32 v86, v92, v93\n\t"
        "v_cvt_pk_bf16_f32 v87, v94, v95\n\t"
        "v_mfma_f32_32x32x16_bf16 v[36:51], v[116:119], v[88:91], v[36:51]\n\t"
        "v_exp_f32 v92, v60\n\t"
        "v_exp_f32 v93, v61\n\t"
        "v_exp_f32 v94, v62\n\t"
        "v_exp_f32 v95, v63\n\t"
        "v_cvt_pk_bf16_f32 v88, v92, v93\n\t"
        "v_cvt_pk_bf16_f32 v89, v94, v95\n\t"
        "v_mfma_f32_32x32x16_bf16 v[20:35], v[100:103], v[84:87], v[20:35]\n\t"
        "v_exp_f32 v92, v64\n\t"
        "v_exp_f32 v93, v65\n\t"
        "v_exp_f32 v94, v66\n\t"
        "v_exp_f32 v95, v67\n\t"
        "v_cvt_pk_bf16_f32 v90, v92, v93\n\t"
        "v_cvt_pk_bf16_f32 v91, v94, v95\n\t"
        "s_waitcnt lgkmcnt(0)\n\t"
        "v_readfirstlane_b32 %[t], v18\n\t"
        "s_nop 0\n\t"
        "s_cmp_ge_u32 %[t], 8\n\t"
        "s_cbranch_scc1 AOK4_%=\n\t"
        "ASP4_%=:\n\t"
        "s_sleep 1\n\t"
        "v_mov_b32 v18, %[cn]\n\t"
        "ds_read_b32 v18, v18 offset:16\n\t"
        "s_waitcnt lgkmcnt(0)\n\t"
        "v_readfirstlane_b32 %[t], v18\n\t"
        "s_nop 0\n\t"
        "s_cmp_ge_u32 %[t], 8\n\t"
        "s_cbranch_scc0 ASP4_%=\n\t"
        "AOK4_%=:\n\t"
        "ds_read_b128 v[96:99], v12 offset:20480\n\t"
        "ds_read_b64 v[120:121], v13 offset:20480\n\t"
        "ds_read_b128 v[112:115], v14 offset:3840\n\t"
        "ds_read_b128 v[116:119], v14 offset:4480\n\t"
        "s_setprio 3\n\t"
        "v_mfma_f32_32x32x16_bf16 v[52:67], v[108:111], v[0:3], 0\n\t"
        "s_add_u32 m0, %[m0], 40960\n\t"
        "v_exp_f32 v92, v68\n\t"
        "v_exp_f32 v93, v69\n\t"
        "global_load_lds_dwordx4 v15, s[40:41] sc1\n\t"
        "v_exp_f32 v94, v70\n\t"
        "v_exp_f32 v95, v71\n\t"
        "v_cvt_pk_bf16_f32 v84, v92, v93\n\t"
        "v_cvt_pk_bf16_f32 v85, v94, v95\n\t"
        "v_mfma_f32_32x32x8_bf16 v[52:67], v[122:123], v[8:9], v[52:67]\n\t"
        "s_add_u32 m0, %[m1], 40960\n\t"
        "v_exp_f32 v92, v72\n\t"
        "v_exp_f32 v93, v73\n\t"
        "global_load_lds_dwordx4 v16, s[40:41] sc1\n\t"
        "v_exp_f32 v94, v74\n\t"
        "v_exp_f32 v95, v75\n\t"
        "v_cvt_pk_bf16_f32 v86, v92, v93\n\t"
        "v_cvt_pk_bf16_f32 v87, v94, v95\n\t"
        "v_mfma_f32_32x32x16_bf16 v[20:35], v[104:107], v[88:91], v[20:35]\n\t"
        "s_add_u32 m0, %[m2], 40960\n\t"
        "v_exp_f32 v92, v76\n\t"
        "v_exp_f32 v93, v77\n\t"
        "global_load_lds_dwordx4 v17, s[40:41] sc1\n\t"
        "v_exp_f32 v94, v78\n\t"
        "v_exp_f32 v95, v79\n\t"
        "v_cvt_pk_bf16_f32 v88, v92, v93\n\t"
        "v_cvt_pk_bf16_f32 v89, v94, v95\n\t"
        "v_mfma_f32_32x32x16_bf16 v[36:51], v[100:103], v[84:87], v[36:51]\n\t"
        "v_exp_f32 v92, v80\n\t"
        "v_exp_f32 v93, v81\n\t"
        "v_exp_f32 v94, v82\n\t"
        "v_exp_f32 v95, v83\n\t"
        "v_cvt_pk_bf16_f32 v90, v92, v93\n\t"
        "v_cvt_pk_bf16_f32 v91, v94, v95\n\t"
        "s_add_u32 s40, s40, 10240\n\t"
        "s_addc_u32 s41, s41, 0\n\t"
        "s_waitcnt lgkmcnt(0)\n\t"
        "v_mfma_f32_32x32x16_bf16 v[68:83], v[108:111], v[4:7], 0\n\t"
        "v_exp_f32 v92, v52\n\t"
        "v_exp_f32 v93, v53\n\t"
        "v_exp_f32 v94, v54\n\t"
        "v_exp_f32 v95, v55\n\t"
        "v_cvt_pk_bf16_f32 v84, v92, v93\n\t"
        "v_cvt_pk_bf16_f32 v85, v94, v95\n\t"
        "v_mfma_f32_32x32x8_bf16 v[68:83], v[122:123], v[10:11], v[68:83]\n\t"
        "v_exp_f32 v92, v56\n\t"
        "v_exp_f32 v93, v57\n\t"
        "v_exp_f32 v94, v58\n\t"
        "v_exp_f32 v95, v59\n\t"
        "v_cvt_pk_bf16_f32 v86, v92, v93\n\t"
        "v_cvt_pk_bf16_f32 v87, v94, v95\n\t"
        "v_mfma_f32_32x32x16_bf16 v[36:51], v[104:107], v[88:91], v[36:51]\n\t"
        "v_exp_f32 v92, v60\n\t"
        "v_exp_f32 v93, v61\n\t"
        "v_exp_f32 v94, v62\n\t"
        "v_exp_f32 v95, v63\n\t"
        "v_cvt_pk_bf16_f32 v88, v92, v93\n\t"
        "v_cvt_pk_bf16_f32 v89, v94, v95\n\t"
        "v_mfma_f32_32x32x16_bf16 v[20:35], v[112:115], v[84:87], v[20:35]\n\t"
        "v_exp_f32 v92, v64\n\t"
        "v_exp_f32 v93, v65\n\t"
        "v_exp_f32 v94, v66\n\t"
        "v_exp_f32 v95, v67\n\t"
        "v_cvt_pk_bf16_f32 v90, v92, v93\n\t"
        "v_cvt_pk_bf16_f32 v91, v94, v95\n\t"
        "ds_read_b128 v[108:111], v12 offset:21504\n\t"
        "ds_read_b64 v[122:123], v13 offset:20736\n\t"
        "ds_read_b128 v[100:103], v14 offset:20480\n\t"
        "ds_read_b128 v[104:107], v14 offset:21120\n\t"
        "s_setprio 2\n\t"
        "v_mfma_f32_32x32x16_bf16 v[52:67], v[96:99], v[0:3], 0\n\t"
        "v_exp_f32 v92, v68\n\t"
        "v_exp_f32 v93, v69\n\t"
        "v_exp_f32 v94, v70\n\t"
        "v_exp_f32 v95, v71\n\t"
        "v_cvt_pk_bf16_f32 v84, v92, v93\n\t"
        "v_cvt_pk_bf16_f32 v85, v94, v95\n\t"
        "v_mfma_f32_32x32x8_bf16 v[52:67], v[120:121], v[8:9], v[52:67]\n\t"
        "v_exp_f32 v92, v72\n\t"
        "v_exp_f32 v93, v73\n\t"
        "v_exp_f32 v94, v74\n\t"
        "v_exp_f32 v95, v75\n\t"
        "v_cvt_pk_bf16_f32 v86, v92, v93\n\t"
        "v_cvt_pk_bf16_f32 v87, v94, v95\n\t"
        "v_mfma_f32_32x32x16_bf16 v[20:35], v[116:119], v[88:91], v[20:35]\n\t"
        "v_exp_f32 v92, v76\n\t"
        "v_exp_f32 v93, v77\n\t"
        "v_exp_f32 v94, v78\n\t"
        "v_exp_f32 v95, v79\n\t"
        "v_cvt_pk_bf16_f32 v88, v92, v93\n\t"
        "v_cvt_pk_bf16_f32 v89, v94, v95\n\t"
        "v_mfma_f32_32x32x16_bf16 v[36:51], v[112:115], v[84:87], v[36:51]\n\t"
        "v_exp_f32 v92, v80\n\t"
        "v_exp_f32 v93, v81\n\t"
        "v_exp_f32 v94, v82\n\t"
        "v_exp_f32 v95, v83\n\t"
        "v_cvt_pk_bf16_f32 v90, v92, v93\n\t"
        "v_cvt_pk_bf16_f32 v91, v94, v95\n\t"
        "s_waitcnt lgkmcnt(0)\n\t"
        "v_mfma_f32_32x32x16_bf16 v[68:83], v[96:99], v[4:7], 0\n\t"
        "v_exp_f32 v92, v52\n\t"
        "v_exp_f32 v93, v53\n\t"
        "v_exp_f32 v94, v54\n\t"
        "v_exp_f32 v95, v55\n\t"
        "v_cvt_pk_bf16_f32 v84, v92, v93\n\t"
        "v_cvt_pk_bf16_f32 v85, v94, v95\n\t"
        "v_mfma_f32_32x32x8_bf16 v[68:83], v[120:121], v[10:11], v[68:83]\n\t"
        "v_exp_f32 v92, v56\n\t"
        "v_exp_f32 v93, v57\n\t"
        "v_exp_f32 v94, v58\n\t"
        "v_exp_f32 v95, v59\n\t"
        "v_cvt_pk_bf16_f32 v86, v92, v93\n\t"
        "v_cvt_pk_bf16_f32 v87, v94, v95\n\t"
        "v_mfma_f32_32x32x16_bf16 v[36:51], v[116:119], v[88:91], v[36:51]\n\t"
        "v_exp_f32 v92, v60\n\t"
        "v_exp_f32 v93, v61\n\t"
        "v_exp_f32 v94, v62\n\t"
        "v_exp_f32 v95, v63\n\t"
        "v_cvt_pk_bf16_f32 v88, v92, v93\n\t"
        "v_cvt_pk_bf16_f32 v89, v94, v95\n\t"
        "v_mfma_f32_32x32x16_bf16 v[20:35], v[100:103], v[84:87], v[20:35]\n\t"
        "v_exp_f32 v92, v64\n\t"
        "v_exp_f32 v93, v65\n\t"
        "v_exp_f32 v94, v66\n\t"
        "v_exp_f32 v95, v67\n\t"
        "v_cvt_pk_bf16_f32 v90, v92, v93\n\t"
        "v_cvt_pk_bf16_f32 v91, v94, v95\n\t"
        "ds_read_b128 v[96:99], v12 offset:22528\n\t"
        "ds_read_b64 v[120:121], v13 offset:20992\n\t"
        "ds_read_b128 v[112:115], v14 offset:21760\n\t"
        "ds_read_b128 v[116:119], v14 offset:22400\n\t"
        "s_setprio 1\n\t"
        "v_mfma_f32_32x32x16_bf16 v[52:67], v[108:111], v[0:3], 0\n\t"
        "v_exp_f32 v92, v68\n\t"
        "v_exp_f32 v93, v69\n\t"
        "v_exp_f32 v94, v70\n\t"
        "v_exp_f32 v95, v71\n\t"
        "v_cvt_pk_bf16_f32 v84, v92, v93\n\t"
        "v_cvt_pk_bf16_f32 v85, v94, v95\n\t"
        "v_mfma_f32_32x32x8_bf16 v[52:67], v[122:123], v[8:9], v[52:67]\n\t"
        "v_exp_f32 v92, v72\n\t"
        "v_exp_f32 v93, v73\n\t"
        "v_exp_f32 v94, v74\n\t"
        "v_exp_f32 v95, v75\n\t"
        "v_cvt_pk_bf16_f32 v86, v92, v93\n\t"
        "v_cvt_pk_bf16_f32 v87, v94, v95\n\t"
        "v_mfma_f32_32x32x16_bf16 v[20:35], v[104:107], v[88:91], v[20:35]\n\t"
        "v_exp_f32 v92, v76\n\t"
        "v_exp_f32 v93, v77\n\t"
        "v_exp_f32 v94, v78\n\t"
        "v_exp_f32 v95, v79\n\t"
        "v_cvt_pk_bf16_f32 v88, v92, v93\n\t"
        "v_cvt_pk_bf16_f32 v89, v94, v95\n\t"
        "v_mfma_f32_32x32x16_bf16 v[36:51], v[100:103], v[84:87], v[36:51]\n\t"
        "v_exp_f32 v92, v80\n\t"
        "v_exp_f32 v93, v81\n\t"
        "v_exp_f32 v94, v82\n\t"
        "v_exp_f32 v95, v83\n\t"
        "v_cvt_pk_bf16_f32 v90, v92, v93\n\t"
        "v_cvt_pk_bf16_f32 v91, v94, v95\n\t"
        "s_waitcnt lgkmcnt(0)\n\t"
        "v_mfma_f32_32x32x16_bf16 v[68:83], v[108:111], v[4:7], 0\n\t"
        "v_exp_f32 v92, v52\n\t"
        "v_exp_f32 v93, v53\n\t"
        "v_exp_f32 v94, v54\n\t"
        "v_exp_f32 v95, v55\n\t"
        "v_cvt_pk_bf16_f32 v84, v92, v93\n\t"
        "v_cvt_pk_bf16_f32 v85, v94, v95\n\t"
        "v_mfma_f32_32x32x8_bf16 v[68:83], v[122:123], v[10:11], v[68:83]\n\t"
        "v_exp_f32 v92, v56\n\t"
        "v_exp_f32 v93, v57\n\t"
        "v_exp_f32 v94, v58\n\t"
        "v_exp_f32 v95, v59\n\t"
        "v_cvt_pk_bf16_f32 v86, v92, v93\n\t"
        "v_cvt_pk_bf16_f32 v87, v94, v95\n\t"
        "v_mfma_f32_32x32x16_bf16 v[36:51], v[104:107], v[88:91], v[36:51]\n\t"
        "v_exp_f32 v92, v60\n\t"
        "v_exp_f32 v93, v61\n\t"
        "v_exp_f32 v94, v62\n\t"
        "v_exp_f32 v95, v63\n\t"
        "v_cvt_pk_bf16_f32 v88, v92, v93\n\t"
        "v_cvt_pk_bf16_f32 v89, v94, v95\n\t"
        "v_mfma_f32_32x32x16_bf16 v[20:35], v[112:115], v[84:87], v[20:35]\n\t"
        "v_exp_f32 v92, v64\n\t"
        "v_exp_f32 v93, v65\n\t"
        "v_exp_f32 v94, v66\n\t"
        "v_exp_f32 v95, v67\n\t"
        "v_cvt_pk_bf16_f32 v90, v92, v93\n\t"
        "v_cvt_pk_bf16_f32 v91, v94, v95\n\t"
        "s_waitcnt vmcnt(0)\n\t"
        "v_mov_b32 v18, %[cn]\n\t"
        "v_mov_b32 v92, 1\n\t"
        "s_mov_b64 exec, 1\n\t"
        "ds_add_u32 v18, v92 offset:20\n\t"
        "s_mov_b64 exec, -1\n\t"
        "ds_read_b128 v[108:111], v12 offset:23552\n\t"
        "ds_read_b64 v[122:123], v13 offset:21248\n\t"
        "ds_read_b128 v[100:103], v14 offset:23040\n\t"
        "ds_read_b128 v[104:107], v14 offset:23680\n\t"
        "s_setprio 0\n\t"
        "v_mfma_f32_32x32x16_bf16 v[52:67], v[96:99], v[0:3], 0\n\t"
        "v_exp_f32 v92, v68\n\t"
        "v_exp_f32 v93, v69\n\t"
        "v_exp_f32 v94, v70\n\t"
        "v_exp_f32 v95, v71\n\t"
        "v_cvt_pk_bf16_f32 v84, v92, v93\n\t"
        "v_cvt_pk_bf16_f32 v85, v94, v95\n\t"
        "v_mfma_f32_32x32x8_bf16 v[52:67], v[120:121], v[8:9], v[52:67]\n\t"
        "v_exp_f32 v92, v72\n\t"
        "v_exp_f32 v93, v73\n\t"
        "v_exp_f32 v94, v74\n\t"
        "v_exp_f32 v95, v75\n\t"
        "v_cvt_pk_bf16_f32 v86, v92, v93\n\t"
        "v_cvt_pk_bf16_f32 v87, v94, v95\n\t"
        "v_mfma_f32_32x32x16_bf16 v[20:35], v[116:119], v[88:91], v[20:35]\n\t"
        "v_exp_f32 v92, v76\n\t"
        "v_exp_f32 v93, v77\n\t"
        "v_exp_f32 v94, v78\n\t"
        "v_exp_f32 v95, v79\n\t"
        "v_cvt_pk_bf16_f32 v88, v92, v93\n\t"
        "v_cvt_pk_bf16_f32 v89, v94, v95\n\t"
        "v_mfma_f32_32x32x16_bf16 v[36:51], v[112:115], v[84:87], v[36:51]\n\t"
        "v_exp_f32 v92, v80\n\t"
        "v_exp_f32 v93, v81\n\t"
        "v_exp_f32 v94, v82\n\t"
        "v_exp_f32 v95, v83\n\t"
        "v_cvt_pk_bf16_f32 v90, v92, v93\n\t"
        "v_cvt_pk_bf16_f32 v91, v94, v95\n\t"
        "s_waitcnt lgkmcnt(0)\n\t"
        "v_mov_b32 v18, %[cn]\n\t"
        "ds_read_b32 v18, v18 offset:20\n\t"
        "v_mfma_f32_32x32x16_bf16 v[68:83], v[96:99], v[4:7], 0\n\t"
        "v_exp_f32 v92, v52\n\t"
        "v_exp_f32 v93, v53\n\t"
        "v_exp_f32 v94, v54\n\t"
        "v_exp_f32 v95, v55\n\t"
        "v_cvt_pk_bf16_f32 v84, v92, v93\n\t"
        "v_cvt_pk_bf16_f32 v85, v94, v95\n\t"
        "v_mfma_f32_32x32x8_bf16 v[68:83], v[120:121], v[10:11], v[68:83]\n\t"
        "v_exp_f32 v92, v56\n\t"
        "v_exp_f32 v93, v57\n\t"
        "v_exp_f32 v94, v58\n\t"
        "v_exp_f32 v95, v59\n\t"
        "v_cvt_pk_bf16_f32 v86, v92, v93\n\t"
        "v_cvt_pk_bf16_f32 v87, v94, v95\n\t"
        "v_mfma_f32_32x32x16_bf16 v[36:51], v[116:119], v[88:91], v[36:51]\n\t"
        "v_exp_f32 v92, v60\n\t"
        "v_exp_f32 v93, v61\n\t"
        "v_exp_f32 v94, v62\n\t"
        "v_exp_f32 v95, v63\n\t"
        "v_cvt_pk_bf16_f32 v88, v92, v93\n\t"
        "v_cvt_pk_bf16_f32 v89, v94, v95\n\t"
        "v_mfma_f32_32x32x16_bf16 v[20:35], v[100:103], v[84:87], v[20:35]\n\t"
        "v_exp_f32 v92, v64\n\t"
        "v_exp_f32 v93, v65\n\t"
        "v_exp_f32 v94, v66\n\t"
        "v_exp_f32 v95, v67\n\t"
        "v_cvt_pk_bf16_f32 v90, v92, v93\n\t"
        "v_cvt_pk_bf16_f32 v91, v94, v95\n\t"
        "s_waitcnt lgkmcnt(0)\n\t"
        "v_readfirstlane_b32 %[t], v18\n\t"
        "s_nop 0\n\t"
        "s_cmp_ge_u32 %[t], 8\n\t"
        "s_cbranch_scc1 AOK5_%=\n\t"
        "ASP5_%=:\n\t"
        "s_sleep 1\n\t"
        "v_mov_b32 v18, %[cn]\n\t"
        "ds_read_b32 v18, v18 offset:20\n\t"
        "s_waitcnt lgkmcnt(0)\n\t"
        "v_readfirstlane_b32 %[t], v18\n\t"
        "s_nop 0\n\t"
        "s_cmp_ge_u32 %[t], 8\n\t"
        "s_cbranch_scc0 ASP5_%=\n\t"
        "AOK5_%=:\n\t"
        "ds_read_b128 v[96:99], v12 offset:40960\n\t"
        "ds_read_b64 v[120:121], v13 offset:40960\n\t"
        "ds_read_b128 v[112:115], v14 offset:24320\n\t"
        "ds_read_b128 v[116:119], v14 offset:24960\n\t"
        "s_setprio 3\n\t"
        "v_mfma_f32_32x32x16_bf16 v[52:67], v[108:111], v[0:3], 0\n\t"
        "s_add_u32 m0, %[m0], 0\n\t"
        "v_exp_f32 v92, v68\n\t"
        "v_exp_f32 v93, v69\n\t"
        "global_load_lds_dwordx4 v15, s[40:41] sc1\n\t"
        "v_exp_f32 v94, v70\n\t"
        "v_exp_f32 v95, v71\n\t"
        "v_cvt_pk_bf16_f32 v84, v92, v93\n\t"
        "v_cvt_pk_bf16_f32 v85, v94, v95\n\t"
        "v_mfma_f32_32x32x8_bf16 v[52:67], v[122:123], v[8:9], v[52:67]\n\t"
        "s_add_u32 m0, %[m1], 0\n\t"
        "v_exp_f32 v92, v72\n\t"
        "v_exp_f32 v93, v73\n\t"
        "global_load_lds_dwordx4 v16, s[40:41] sc1\n\t"
        "v_exp_f32 v94, v74\n\t"
        "v_exp_f32 v95, v75\n\t"
        "v_cvt_pk_bf16_f32 v86, v92, v93\n\t"
        "v_cvt_pk_bf16_f32 v87, v94, v95\n\t"
        "v_mfma_f32_32x32x16_bf16 v[20:35], v[104:107], v[88:91], v[20:35]\n\t"
        "s_add_u32 m0, %[m2], 0\n\t"
        "v_exp_f32 v92, v76\n\t"
        "v_exp_f32 v93, v77\n\t"
        "global_load_lds_dwordx4 v17, s[40:41] sc1\n\t"
        "v_exp_f32 v94, v78\n\t"
        "v_exp_f32 v95, v79\n\t"
        "v_cvt_pk_bf16_f32 v88, v92, v93\n\t"
        "v_cvt_pk_bf16_f32 v89, v94, v95\n\t"
        "v_mfma_f32_32x32x16_bf16 v[36:51], v[100:103], v[84:87], v[36:51]\n\t"
        "v_exp_f32 v92, v80\n\t"
        "v_exp_f32 v93, v81\n\t"
        "v_exp_f32 v94, v82\n\t"
        "v_exp_f32 v95, v83\n\t"
        "v_cvt_pk_bf16_f32 v90, v92, v93\n\t"
        "v_cvt_pk_bf16_f32 v91, v94, v95\n\t"
        "s_add_u32 s40, s40, 10240\n\t"
        "s_addc_u32 s41, s41, 0\n\t"
        "s_waitcnt lgkmcnt(0)\n\t"
        "v_mfma_f32_32x32x16_bf16 v[68:83], v[108:111], v[4:7], 0\n\t"
        "v_exp_f32 v92, v52\n\t"
        "v_exp_f32 v93, v53\n\t"
        "v_exp_f32 v94, v54\n\t"
        "v_exp_f32 v95, v55\n\t"
        "v_cvt_pk_bf16_f32 v84, v92, v93\n\t"
        "v_cvt_pk_bf16_f32 v85, v94, v95\n\t"
        "v_mfma_f32_32x32x8_bf16 v[68:83], v[122:123], v[10:11], v[68:83]\n\t"
        "v_exp_f32 v92, v56\n\t"
        "v_exp_f32 v93, v57\n\t"
        "v_exp_f32 v94, v58\n\t"
        "v_exp_f32 v95, v59\n\t"
        "v_cvt_pk_bf16_f32 v86, v92, v93\n\t"
        "v_cvt_pk_bf16_f32 v87, v94, v95\n\t"
        "v_mfma_f32_32x32x16_bf16 v[36:51], v[104:107], v[88:91], v[36:51]\n\t"
        "v_exp_f32 v92, v60\n\t"
        "v_exp_f32 v93, v61\n\t"
        "v_exp_f32 v94, v62\n\t"
        "v_exp_f32 v95, v63\n\t"
        "v_cvt_pk_bf16_f32 v88, v92, v93\n\t"
        "v_cvt_pk_bf16_f32 v89, v94, v95\n\t"
        "v_mfma_f32_32x32x16_bf16 v[20:35], v[112:115], v[84:87], v[20:35]\n\t"
        "v_exp_f32 v92, v64\n\t"
        "v_exp_f32 v93, v65\n\t"
        "v_exp_f32 v94, v66\n\t"
        "v_exp_f32 v95, v67\n\t"
        "v_cvt_pk_bf16_f32 v90, v92, v93\n\t"
        "v_cvt_pk_bf16_f32 v91, v94, v95\n\t"
        "ds_read_b128 v[108:111], v12 offset:41984\n\t"
        "ds_read_b64 v[122:123], v13 offset:41216\n\t"
        "ds_read_b128 v[100:103], v14 offset:40960\n\t"
        "ds_read_b128 v[104:107], v14 offset:41600\n\t"
        "s_setprio 2\n\t"
        "v_mfma_f32_32x32x16_bf16 v[52:67], v[96:99], v[0:3], 0\n\t"
        "v_exp_f32 v92, v68\n\t"
        "v_exp_f32 v93, v69\n\t"
        "v_exp_f32 v94, v70\n\t"
        "v_exp_f32 v95, v71\n\t"
        "v_cvt_pk_bf16_f32 v84, v92, v93\n\t"
        "v_cvt_pk_bf16_f32 v85, v94, v95\n\t"
        "v_mfma_f32_32x32x8_bf16 v[52:67], v[120:121], v[8:9], v[52:67]\n\t"
        "v_exp_f32 v92, v72\n\t"
        "v_exp_f32 v93, v73\n\t"
        "v_exp_f32 v94, v74\n\t"
        "v_exp_f32 v95, v75\n\t"
        "v_cvt_pk_bf16_f32 v86, v92, v93\n\t"
        "v_cvt_pk_bf16_f32 v87, v94, v95\n\t"
        "v_mfma_f32_32x32x16_bf16 v[20:35], v[116:119], v[88:91], v[20:35]\n\t"
        "v_exp_f32 v92, v76\n\t"
        "v_exp_f32 v93, v77\n\t"
        "v_exp_f32 v94, v78\n\t"
        "v_exp_f32 v95, v79\n\t"
        "v_cvt_pk_bf16_f32 v88, v92, v93\n\t"
        "v_cvt_pk_bf16_f32 v89, v94, v95\n\t"
        "v_mfma_f32_32x32x16_bf16 v[36:51], v[112:115], v[84:87], v[36:51]\n\t"
        "v_exp_f32 v92, v80\n\t"
        "v_exp_f32 v93, v81\n\t"
        "v_exp_f32 v94, v82\n\t"
        "v_exp_f32 v95, v83\n\t"
        "v_cvt_pk_bf16_f32 v90, v92, v93\n\t"
        "v_cvt_pk_bf16_f32 v91, v94, v95\n\t"
        "s_waitcnt lgkmcnt(0)\n\t"
        "v_mfma_f32_32x32x16_bf16 v[68:83], v[96:99], v[4:7], 0\n\t"
        "v_exp_f32 v92, v52\n\t"
        "v_exp_f32 v93, v53\n\t"
        "v_exp_f32 v94, v54\n\t"
        "v_exp_f32 v95, v55\n\t"
        "v_cvt_pk_bf16_f32 v84, v92, v93\n\t"
        "v_cvt_pk_bf16_f32 v85, v94, v95\n\t"
        "v_mfma_f32_32x32x8_bf16 v[68:83], v[120:121], v[10:11], v[68:83]\n\t"
        "v_exp_f32 v92, v56\n\t"
        "v_exp_f32 v93, v57\n\t"
        "v_exp_f32 v94, v58\n\t"
        "v_exp_f32 v95, v59\n\t"
        "v_cvt_pk_bf16_f32 v86, v92, v93\n\t"
        "v_cvt_pk_bf16_f32 v87, v94, v95\n\t"
        "v_mfma_f32_32x32x16_bf16 v[36:51], v[116:119], v[88:91], v[36:51]\n\t"
        "v_exp_f32 v92, v60\n\t"
        "v_exp_f32 v93, v61\n\t"
        "v_exp_f32 v94, v62\n\t"
        "v_exp_f32 v95, v63\n\t"
        "v_cvt_pk_bf16_f32 v88, v92, v93\n\t"
        "v_cvt_pk_bf16_f32 v89, v94, v95\n\t"
        "v_mfma_f32_32x32x16_bf16 v[20:35], v[100:103], v[84:87], v[20:35]\n\t"
        "v_exp_f32 v92, v64\n\t"
        "v_exp_f32 v93, v65\n\t"
        "v_exp_f32 v94, v66\n\t"
        "v_exp_f32 v95, v67\n\t"
        "v_cvt_pk_bf16_f32 v90, v92, v93\n\t"
        "v_cvt_pk_bf16_f32 v91, v94, v95\n\t"
        "ds_read_b128 v[96:99], v12 offset:43008\n\t"
        "ds_read_b64 v[120:121], v13 offset:41472\n\t"
        "ds_read_b128 v[112:115], v14 offset:42240\n\t"
        "ds_read_b128 v[116:119], v14 offset:42880\n\t"
        "s_setprio 1\n\t"
        "v_mfma_f32_32x32x16_bf16 v[52:67], v[108:111], v[0:3], 0\n\t"
        "v_exp_f32 v92, v68\n\t"
        "v_exp_f32 v93, v69\n\t"
        "v_exp_f32 v94, v70\n\t"
        "v_exp_f32 v95, v71\n\t"
        "v_cvt_pk_bf16_f32 v84, v92, v93\n\t"
        "v_cvt_pk_bf16_f32 v85, v94, v95\n\t"
        "v_mfma_f32_32x32x8_bf16 v[52:67], v[122:123], v[8:9], v[52:67]\n\t"
        "v_exp_f32 v92, v72\n\t"
        "v_exp_f32 v93, v73\n\t"
        "v_exp_f32 v94, v74\n\t"
        "v_exp_f32 v95, v75\n\t"
        "v_cvt_pk_bf16_f32 v86, v92, v93\n\t"
        "v_cvt_pk_bf16_f32 v87, v94, v95\n\t"
        "v_mfma_f32_32x32x16_bf16 v[20:35], v[104:107], v[88:91], v[20:35]\n\t"
        "v_exp_f32 v92, v76\n\t"
        "v_exp_f32 v93, v77\n\t"
        "v_exp_f32 v94, v78\n\t"
        "v_exp_f32 v95, v79\n\t"
        "v_cvt_pk_bf16_f32 v88, v92, v93\n\t"
        "v_cvt_pk_bf16_f32 v89, v94, v95\n\t"
        "v_mfma_f32_32x32x16_bf16 v[36:51], v[100:103], v[84:87], v[36:51]\n\t"
        "v_exp_f32 v92, v80\n\t"
        "v_exp_f32 v93, v81\n\t"
        "v_exp_f32 v94, v82\n\t"
        "v_exp_f32 v95, v83\n\t"
        "v_cvt_pk_bf16_f32 v90, v92, v93\n\t"
        "v_cvt_pk_bf16_f32 v91, v94, v95\n\t"
        "s_waitcnt lgkmcnt(0)\n\t"
        "v_mfma_f32_32x32x16_bf16 v[68:83], v[108:111], v[4:7], 0\n\t"
        "v_exp_f32 v92, v52\n\t"
        "v_exp_f32 v93, v53\n\t"
        "v_exp_f32 v94, v54\n\t"
        "v_exp_f32 v95, v55\n\t"
        "v_cvt_pk_bf16_f32 v84, v92, v93\n\t"
        "v_cvt_pk_bf16_f32 v85, v94, v95\n\t"
        "v_mfma_f32_32x32x8_bf16 v[68:83], v[122:123], v[10:11], v[68:83]\n\t"
        "v_exp_f32 v92, v56\n\t"
        "v_exp_f32 v93, v57\n\t"
        "v_exp_f32 v94, v58\n\t"
        "v_exp_f32 v95, v59\n\t"
        "v_cvt_pk_bf16_f32 v86, v92, v93\n\t"
        "v_cvt_pk_bf16_f32 v87, v94, v95\n\t"
        "v_mfma_f32_32x32x16_bf16 v[36:51], v[104:107], v[88:91], v[36:51]\n\t"
        "v_exp_f32 v92, v60\n\t"
        "v_exp_f32 v93, v61\n\t"
        "v_exp_f32 v94, v62\n\t"
        "v_exp_f32 v95, v63\n\t"
        "v_cvt_pk_bf16_f32 v88, v92, v93\n\t"
        "v_cvt_pk_bf16_f32 v89, v94, v95\n\t"
        "v_mfma_f32_32x32x16_bf16 v[20:35], v[112:115], v[84:87], v[20:35]\n\t"
        "v_exp_f32 v92, v64\n\t"
        "v_exp_f32 v93, v65\n\t"
        "v_exp_f32 v94, v66\n\t"
        "v_exp_f32 v95, v67\n\t"
        "v_cvt_pk_bf16_f32 v90, v92, v93\n\t"
        "v_cvt_pk_bf16_f32 v91, v94, v95\n\t"
        "s_waitcnt vmcnt(0)\n\t"
        "v_mov_b32 v18, %[cn]\n\t"
        "v_mov_b32 v92, 1\n\t"
        "s_mov_b64 exec, 1\n\t"
        "ds_add_u32 v18, v92 offset:24\n\t"
        "s_mov_b64 exec, -1\n\t"
        "ds_read_b128 v[108:111], v12 offset:44032\n\t"
        "ds_read_b64 v[122:123], v13 offset:41728\n\t"
        "ds_read_b128 v[100:103], v14 offset:43520\n\t"
        "ds_read_b128 v[104:107], v14 offset:44160\n\t"
        "s_setprio 0\n\t"
        "v_mfma_f32_32x32x16_bf16 v[52:67], v[96:99], v[0:3], 0\n\t"
        "v_exp_f32 v92, v68\n\t"
        "v_exp_f32 v93, v69\n\t"
        "v_exp_f32 v94, v70\n\t"
        "v_exp_f32 v95, v71\n\t"
        "v_cvt_pk_bf16_f32 v84, v92, v93\n\t"
        "v_cvt_pk_bf16_f32 v85, v94, v95\n\t"
        "v_mfma_f32_32x32x8_bf16 v[52:67], v[120:121], v[8:9], v[52:67]\n\t"
        "v_exp_f32 v92, v72\n\t"
        "v_exp_f32 v93, v73\n\t"
        "v_exp_f32 v94, v74\n\t"
        "v_exp_f32 v95, v75\n\t"
        "v_cvt_pk_bf16_f32 v86, v92, v93\n\t"
        "v_cvt_pk_bf16_f32 v87, v94, v95\n\t"
        "v_mfma_f32_32x32x16_bf16 v[20:35], v[116:119], v[88:91], v[20:35]\n\t"
        "v_exp_f32 v92, v76\n\t"
        "v_exp_f32 v93, v77\n\t"
        "v_exp_f32 v94, v78\n\t"
        "v_exp_f32 v95, v79\n\t"
        "v_cvt_pk_bf16_f32 v88, v92, v93\n\t"
        "v_cvt_pk_bf16_f32 v89, v94, v95\n\t"
        "v_mfma_f32_32x32x16_bf16 v[36:51], v[112:115], v[84:87], v[36:51]\n\t"
        "v_exp_f32 v92, v80\n\t"
        "v_exp_f32 v93, v81\n\t"
        "v_exp_f32 v94, v82\n\t"
        "v_exp_f32 v95, v83\n\t"
        "v_cvt_pk_bf16_f32 v90, v92, v93\n\t"
        "v_cvt_pk_bf16_f32 v91, v94, v95\n\t"
        "s_waitcnt lgkmcnt(0)\n\t"
        "v_mov_b32 v18, %[cn]\n\t"
        "ds_read_b32 v18, v18 offset:24\n\t"
        "v_mfma_f32_32x32x16_bf16 v[68:83], v[96:99], v[4:7], 0\n\t"
        "v_exp_f32 v92, v52\n\t"
        "v_exp_f32 v93, v53\n\t"
        "v_exp_f32 v94, v54\n\t"
        "v_exp_f32 v95, v55\n\t"
        "v_cvt_pk_bf16_f32 v84, v92, v93\n\t"
        "v_cvt_pk_bf16_f32 v85, v94, v95\n\t"
        "v_mfma_f32_32x32x8_bf16 v[68:83], v[120:121], v[10:11], v[68:83]\n\t"
        "v_exp_f32 v92, v56\n\t"
        "v_exp_f32 v93, v57\n\t"
        "v_exp_f32 v94, v58\n\t"
        "v_exp_f32 v95, v59\n\t"
        "v_cvt_pk_bf16_f32 v86, v92, v93\n\t"
        "v_cvt_pk_bf16_f32 v87, v94, v95\n\t"
        "v_mfma_f32_32x32x16_bf16 v[36:51], v[116:119], v[88:91], v[36:51]\n\t"
        "v_exp_f32 v92, v60\n\t"
        "v_exp_f32 v93, v61\n\t"
        "v_exp_f32 v94, v62\n\t"
        "v_exp_f32 v95, v63\n\t"
        "v_cvt_pk_bf16_f32 v88, v92, v93\n\t"
        "v_cvt_pk_bf16_f32 v89, v94, v95\n\t"
        "v_mfma_f32_32x32x16_bf16 v[20:35], v[100:103], v[84:87], v[20:35]\n\t"
        "v_exp_f32 v92, v64\n\t"
        "v_exp_f32 v93, v65\n\t"
        "v_exp_f32 v94, v66\n\t"
        "v_exp_f32 v95, v67\n\t"
        "v_cvt_pk_bf16_f32 v90, v92, v93\n\t"
        "v_cvt_pk_bf16_f32 v91, v94, v95\n\t"
        "s_waitcnt lgkmcnt(0)\n\t"
        "v_readfirstlane_b32 %[t], v18\n\t"
        "s_nop 0\n\t"
        "s_cmp_ge_u32 %[t], 8\n\t"
        "s_cbranch_scc1 AOK6_%=\n\t"
        "ASP6_%=:\n\t"
        "s_sleep 1\n\t"
        "v_mov_b32 v18, %[cn]\n\t"
        "ds_read_b32 v18, v18 offset:24\n\t"
        "s_waitcnt lgkmcnt(0)\n\t"
        "v_readfirstlane_b32 %[t], v18\n\t"
        "s_nop 0\n\t"
        "s_cmp_ge_u32 %[t], 8\n\t"
        "s_cbranch_scc0 ASP6_%=\n\t"
        "AOK6_%=:\n\t"
        "ds_read_b128 v[96:99], v12 offset:0\n\t"
        "ds_read_b64 v[120:121], v13 offset:0\n\t"
        "ds_read_b128 v[112:115], v14 offset:44800\n\t"
        "ds_read_b128 v[116:119], v14 offset:45440\n\t"
        "s_setprio 3\n\t"
        "v_mfma_f32_32x32x16_bf16 v[52:67], v[108:111], v[0:3], 0\n\t"
        "s_add_u32 m0, %[m0], 20480\n\t"
        "v_exp_f32 v92, v68\n\t"
        "v_exp_f32 v93, v69\n\t"
        "global_load_lds_dwordx4 v15, s[40:41] sc1\n\t"
        "v_exp_f32 v94, v70\n\t"
        "v_exp_f32 v95, v71\n\t"
        "v_cvt_pk_bf16_f32 v84, v92, v93\n\t"
        "v_cvt_pk_bf16_f32 v85, v94, v95\n\t"
        "v_mfma_f32_32x32x8_bf16 v[52:67], v[122:123], v[8:9], v[52:67]\n\t"
        "s_add_u32 m0, %[m1], 20480\n\t"
        "v_exp_f32 v92, v72\n\t"
        "v_exp_f32 v93, v73\n\t"
        "global_load_lds_dwordx4 v16, s[40:41] sc1\n\t"
        "v_exp_f32 v94, v74\n\t"
        "v_exp_f32 v95, v75\n\t"
        "v_cvt_pk_bf16_f32 v86, v92, v93\n\t"
        "v_cvt_pk_bf16_f32 v87, v94, v95\n\t"
        "v_mfma_f32_32x32x16_bf16 v[20:35], v[104:107], v[88:91], v[20:35]\n\t"
        "s_add_u32 m0, %[m2], 20480\n\t"
        "v_exp_f32 v92, v76\n\t"
        "v_exp_f32 v93, v77\n\t"
        "global_load_lds_dwordx4 v17, s[40:41] sc1\n\t"
        "v_exp_f32 v94, v78\n\t"
        "v_exp_f32 v95, v79\n\t"
        "v_cvt_pk_bf16_f32 v88, v92, v93\n\t"
        "v_cvt_pk_bf16_f32 v89, v94, v95\n\t"
        "v_mfma_f32_32x32x16_bf16 v[36:51], v[100:103], v[84:87], v[36:51]\n\t"
        "v_exp_f32 v92, v80\n\t"
        "v_exp_f32 v93, v81\n\t"
        "v_exp_f32 v94, v82\n\t"
        "v_exp_f32 v95, v83\n\t"
        "v_cvt_pk_bf16_f32 v90, v92, v93\n\t"
        "v_cvt_pk_bf16_f32 v91, v94, v95\n\t"
        "s_add_u32 s40, s40, 10240\n\t"
        "s_addc_u32 s41, s41, 0\n\t"
        "s_waitcnt lgkmcnt(0)\n\t"
        "v_mfma_f32_32x32x16_bf16 v[68:83], v[108:111], v[4:7], 0\n\t"
        "v_exp_f32 v92, v52\n\t"
        "v_exp_f32 v93, v53\n\t"
        "v_exp_f32 v94, v54\n\t"
        "v_exp_f32 v95, v55\n\t"
        "v_cvt_pk_bf16_f32 v84, v92, v93\n\t"
        "v_cvt_pk_bf16_f32 v85, v94, v95\n\t"
        "v_mfma_f32_32x32x8_bf16 v[68:83], v[122:123], v[10:11], v[68:83]\n\t"
        "v_exp_f32 v92, v56\n\t"
        "v_exp_f32 v93, v57\n\t"
        "v_exp_f32 v94, v58\n\t"
        "v_exp_f32 v95, v59\n\t"
        "v_cvt_pk_bf16_f32 v86, v92, v93\n\t"
        "v_cvt_pk_bf16_f32 v87, v94, v95\n\t"
        "v_mfma_f32_32x32x16_bf16 v[36:51], v[104:107], v[88:91], v[36:51]\n\t"
        "v_exp_f32 v92, v60\n\t"
        "v_exp_f32 v93, v61\n\t"
        "v_exp_f32 v94, v62\n\t"
        "v_exp_f32 v95, v63\n\t"
        "v_cvt_pk_bf16_f32 v88, v92, v93\n\t"
        "v_cvt_pk_bf16_f32 v89, v94, v95\n\t"
        "v_mfma_f32_32x32x16_bf16 v[20:35], v[112:115], v[84:87], v[20:35]\n\t"
        "v_exp_f32 v92, v64\n\t"
        "v_exp_f32 v93, v65\n\t"
        "v_exp_f32 v94, v66\n\t"
        "v_exp_f32 v95, v67\n\t"
        "v_cvt_pk_bf16_f32 v90, v92, v93\n\t"
        "v_cvt_pk_bf16_f32 v91, v94, v95\n\t"
        "ds_read_b128 v[108:111], v12 offset:1024\n\t"
        "ds_read_b64 v[122:123], v13 offset:256\n\t"
        "ds_read_b128 v[100:103], v14 offset:0\n\t"
        "ds_read_b128 v[104:107], v14 offset:640\n\t"
        "s_setprio 2\n\t"
        "v_mfma_f32_32x32x16_bf16 v[52:67], v[96:99], v[0:3], 0\n\t"
        "v_exp_f32 v92, v68\n\t"
        "v_exp_f32 v93, v69\n\t"
        "v_exp_f32 v94, v70\n\t"
        "v_exp_f32 v95, v71\n\t"
        "v_cvt_pk_bf16_f32 v84, v92, v93\n\t"
        "v_cvt_pk_bf16_f32 v85, v94, v95\n\t"
        "v_mfma_f32_32x32x8_bf16 v[52:67], v[120:121], v[8:9], v[52:67]\n\t"
        "v_exp_f32 v92, v72\n\t"
        "v_exp_f32 v93, v73\n\t"
        "v_exp_f32 v94, v74\n\t"
        "v_exp_f32 v95, v75\n\t"
        "v_cvt_pk_bf16_f32 v86, v92, v93\n\t"
        "v_cvt_pk_bf16_f32 v87, v94, v95\n\t"
        "v_mfma_f32_32x32x16_bf16 v[20:35], v[116:119], v[88:91], v[20:35]\n\t"
        "v_exp_f32 v92, v76\n\t"
        "v_exp_f32 v93, v77\n\t"
        "v_exp_f32 v94, v78\n\t"
        "v_exp_f32 v95, v79\n\t"
        "v_cvt_pk_bf16_f32 v88, v92, v93\n\t"
        "v_cvt_pk_bf16_f32 v89, v94, v95\n\t"
        "v_mfma_f32_32x32x16_bf16 v[36:51], v[112:115], v[84:87], v[36:51]\n\t"
        "v_exp_f32 v92, v80\n\t"
        "v_exp_f32 v93, v81\n\t"
        "v_exp_f32 v94, v82\n\t"
        "v_exp_f32 v95, v83\n\t"
        "v_cvt_pk_bf16_f32 v90, v92, v93\n\t"
        "v_cvt_pk_bf16_f32 v91, v94, v95\n\t"
        "s_waitcnt lgkmcnt(0)\n\t"
        "v_mfma_f32_32x32x16_bf16 v[68:83], v[96:99], v[4:7], 0\n\t"
        "v_exp_f32 v92, v52\n\t"
        "v_exp_f32 v93, v53\n\t"
        "v_exp_f32 v94, v54\n\t"
        "v_exp_f32 v95, v55\n\t"
        "v_cvt_pk_bf16_f32 v84, v92, v93\n\t"
        "v_cvt_pk_bf16_f32 v85, v94, v95\n\t"
        "v_mfma_f32_32x32x8_bf16 v[68:83], v[120:121], v[10:11], v[68:83]\n\t"
        "v_exp_f32 v92, v56\n\t"
        "v_exp_f32 v93, v57\n\t"
        "v_exp_f32 v94, v58\n\t"
        "v_exp_f32 v95, v59\n\t"
        "v_cvt_pk_bf16_f32 v86, v92, v93\n\t"
        "v_cvt_pk_bf16_f32 v87, v94, v95\n\t"
        "v_mfma_f32_32x32x16_bf16 v[36:51], v[116:119], v[88:91], v[36:51]\n\t"
        "v_exp_f32 v92, v60\n\t"
        "v_exp_f32 v93, v61\n\t"
        "v_exp_f32 v94, v62\n\t"
        "v_exp_f32 v95, v63\n\t"
        "v_cvt_pk_bf16_f32 v88, v92, v93\n\t"
        "v_cvt_pk_bf16_f32 v89, v94, v95\n\t"
        "v_mfma_f32_32x32x16_bf16 v[20:35], v[100:103], v[84:87], v[20:35]\n\t"
        "v_exp_f32 v92, v64\n\t"
        "v_exp_f32 v93, v65\n\t"
        "v_exp_f32 v94, v66\n\t"
        "v_exp_f32 v95, v67\n\t"
        "v_cvt_pk_bf16_f32 v90, v92, v93\n\t"
        "v_cvt_pk_bf16_f32 v91, v94, v95\n\t"
        "ds_read_b128 v[96:99], v12 offset:2048\n\t"
        "ds_read_b64 v[120:121], v13 offset:512\n\t"
        "ds_read_b128 v[112:115], v14 offset:1280\n\t"
        "ds_read_b128 v[116:119], v14 offset:1920\n\t"
        "s_setprio 1\n\t"
        "v_mfma_f32_32x32x16_bf16 v[52:67], v[108:111], v[0:3], 0\n\t"
        "v_exp_f32 v92, v68\n\t"
        "v_exp_f32 v93, v69\n\t"
        "v_exp_f32 v94, v70\n\t"
        "v_exp_f32 v95, v71\n\t"
        "v_cvt_pk_bf16_f32 v84, v92, v93\n\t"
        "v_cvt_pk_bf16_f32 v85, v94, v95\n\t"
        "v_mfma_f32_32x32x8_bf16 v[52:67], v[122:123], v[8:9], v[52:67]\n\t"
        "v_exp_f32 v92, v72\n\t"
        "v_exp_f32 v93, v73\n\t"
        "v_exp_f32 v94, v74\n\t"
        "v_exp_f32 v95, v75\n\t"
        "v_cvt_pk_bf16_f32 v86, v92, v93\n\t"
        "v_cvt_pk_bf16_f32 v87, v94, v95\n\t"
        "v_mfma_f32_32x32x16_bf16 v[20:35], v[104:107], v[88:91], v[20:35]\n\t"
        "v_exp_f32 v92, v76\n\t"
        "v_exp_f32 v93, v77\n\t"
        "v_exp_f32 v94, v78\n\t"
        "v_exp_f32 v95, v79\n\t"
        "v_cvt_pk_bf16_f32 v88, v92, v93\n\t"
        "v_cvt_pk_bf16_f32 v89, v94, v95\n\t"
        "v_mfma_f32_32x32x16_bf16 v[36:51], v[100:103], v[84:87], v[36:51]\n\t"
        "v_exp_f32 v92, v80\n\t"
        "v_exp_f32 v93, v81\n\t"
        "v_exp_f32 v94, v82\n\t"
        "v_exp_f32 v95, v83\n\t"
        "v_cvt_pk_bf16_f32 v90, v92, v93\n\t"
        "v_cvt_pk_bf16_f32 v91, v94, v95\n\t"
        "s_waitcnt lgkmcnt(0)\n\t"
        "v_mfma_f32_32x32x16_bf16 v[68:83], v[108:111], v[4:7], 0\n\t"
        "v_exp_f32 v92, v52\n\t"
        "v_exp_f32 v93, v53\n\t"
        "v_exp_f32 v94, v54\n\t"
        "v_exp_f32 v95, v55\n\t"
        "v_cvt_pk_bf16_f32 v84, v92, v93\n\t"
        "v_cvt_pk_bf16_f32 v85, v94, v95\n\t"
        "v_mfma_f32_32x32x8_bf16 v[68:83], v[122:123], v[10:11], v[68:83]\n\t"
        "v_exp_f32 v92, v56\n\t"
        "v_exp_f32 v93, v57\n\t"
        "v_exp_f32 v94, v58\n\t"
        "v_exp_f32 v95, v59\n\t"
        "v_cvt_pk_bf16_f32 v86, v92, v93\n\t"
        "v_cvt_pk_bf16_f32 v87, v94, v95\n\t"
        "v_mfma_f32_32x32x16_bf16 v[36:51], v[104:107], v[88:91], v[36:51]\n\t"
        "v_exp_f32 v92, v60\n\t"
        "v_exp_f32 v93, v61\n\t"
        "v_exp_f32 v94, v62\n\t"
        "v_exp_f32 v95, v63\n\t"
        "v_cvt_pk_bf16_f32 v88, v92, v93\n\t"
        "v_cvt_pk_bf16_f32 v89, v94, v95\n\t"
        "v_mfma_f32_32x32x16_bf16 v[20:35], v[112:115], v[84:87], v[20:35]\n\t"
        "v_exp_f32 v92, v64\n\t"
        "v_exp_f32 v93, v65\n\t"
        "v_exp_f32 v94, v66\n\t"
        "v_exp_f32 v95, v67\n\t"
        "v_cvt_pk_bf16_f32 v90, v92, v93\n\t"
        "v_cvt_pk_bf16_f32 v91, v94, v95\n\t"
        "s_waitcnt vmcnt(0)\n\t"
        "v_mov_b32 v18, %[cn]\n\t"
        "v_mov_b32 v92, 1\n\t"
        "s_mov_b64 exec, 1\n\t"
        "ds_add_u32 v18, v92 offset:28\n\t"
        "s_mov_b64 exec, -1\n\t"
        "ds_read_b128 v[108:111], v12 offset:3072\n\t"
        "ds_read_b64 v[122:123], v13 offset:768\n\t"
        "ds_read_b128 v[100:103], v14 offset:2560\n\t"
        "ds_read_b128 v[104:107], v14 offset:3200\n\t"
        "s_setprio 0\n\t"
        "v_mfma_f32_32x32x16_bf16 v[52:67], v[96:99], v[0:3], 0\n\t"
        "v_exp_f32 v92, v68\n\t"
        "v_exp_f32 v93, v69\n\t"
        "v_exp_f32 v94, v70\n\t"
        "v_exp_f32 v95, v71\n\t"
        "v_cvt_pk_bf16_f32 v84, v92, v93\n\t"
        "v_cvt_pk_bf16_f32 v85, v94, v95\n\t"
        "v_mfma_f32_32x32x8_bf16 v[52:67], v[120:121], v[8:9], v[52:67]\n\t"
        "v_exp_f32 v92, v72\n\t"
        "v_exp_f32 v93, v73\n\t"
        "v_exp_f32 v94, v74\n\t"
        "v_exp_f32 v95, v75\n\t"
        "v_cvt_pk_bf16_f32 v86, v92, v93\n\t"
        "v_cvt_pk_bf16_f32 v87, v94, v95\n\t"
        "v_mfma_f32_32x32x16_bf16 v[20:35], v[116:119], v[88:91], v[20:35]\n\t"
        "v_exp_f32 v92, v76\n\t"
        "v_exp_f32 v93, v77\n\t"
        "v_exp_f32 v94, v78\n\t"
        "v_exp_f32 v95, v79\n\t"
        "v_cvt_pk_bf16_f32 v88, v92, v93\n\t"
        "v_cvt_pk_bf16_f32 v89, v94, v95\n\t"
        "v_mfma_f32_32x32x16_bf16 v[36:51], v[112:115], v[84:87], v[36:51]\n\t"
        "v_exp_f32 v92, v80\n\t"
        "v_exp_f32 v93, v81\n\t"
        "v_exp_f32 v94, v82\n\t"
        "v_exp_f32 v95, v83\n\t"
        "v_cvt_pk_bf16_f32 v90, v92, v93\n\t"
        "v_cvt_pk_bf16_f32 v91, v94, v95\n\t"
        "s_waitcnt lgkmcnt(0)\n\t"
        "v_mov_b32 v18, %[cn]\n\t"
        "ds_read_b32 v18, v18 offset:28\n\t"
        "v_mfma_f32_32x32x16_bf16 v[68:83], v[96:99], v[4:7], 0\n\t"
        "v_exp_f32 v92, v52\n\t"
        "v_exp_f32 v93, v53\n\t"
        "v_exp_f32 v94, v54\n\t"
        "v_exp_f32 v95, v55\n\t"
        "v_cvt_pk_bf16_f32 v84, v92, v93\n\t"
        "v_cvt_pk_bf16_f32 v85, v94, v95\n\t"
        "v_mfma_f32_32x32x8_bf16 v[68:83], v[120:121], v[10:11], v[68:83]\n\t"
        "v_exp_f32 v92, v56\n\t"
        "v_exp_f32 v93, v57\n\t"
        "v_exp_f32 v94, v58\n\t"
        "v_exp_f32 v95, v59\n\t"
        "v_cvt_pk_bf16_f32 v86, v92, v93\n\t"
        "v_cvt_pk_bf16_f32 v87, v94, v95\n\t"
        "v_mfma_f32_32x32x16_bf16 v[36:51], v[116:119], v[88:91], v[36:51]\n\t"
        "v_exp_f32 v92, v60\n\t"
        "v_exp_f32 v93, v61\n\t"
        "v_exp_f32 v94, v62\n\t"
        "v_exp_f32 v95, v63\n\t"
        "v_cvt_pk_bf16_f32 v88, v92, v93\n\t"
        "v_cvt_pk_bf16_f32 v89, v94, v95\n\t"
        "v_mfma_f32_32x32x16_bf16 v[20:35], v[100:103], v[84:87], v[20:35]\n\t"
        "v_exp_f32 v92, v64\n\t"
        "v_exp_f32 v93, v65\n\t"
        "v_exp_f32 v94, v66\n\t"
        "v_exp_f32 v95, v67\n\t"
        "v_cvt_pk_bf16_f32 v90, v92, v93\n\t"
        "v_cvt_pk_bf16_f32 v91, v94, v95\n\t"
        "s_waitcnt lgkmcnt(0)\n\t"
        "v_readfirstlane_b32 %[t], v18\n\t"
        "s_nop 0\n\t"
        "s_cmp_ge_u32 %[t], 8\n\t"
        "s_cbranch_scc1 AOK7_%=\n\t"
        "ASP7_%=:\n\t"
        "s_sleep 1\n\t"
        "v_mov_b32 v18, %[cn]\n\t"
        "ds_read_b32 v18, v18 offset:28\n\t"
        "s_waitcnt lgkmcnt(0)\n\t"
        "v_readfirstlane_b32 %[t], v18\n\t"
        "s_nop 0\n\t"
        "s_cmp_ge_u32 %[t], 8\n\t"
        "s_cbranch_scc0 ASP7_%=\n\t"
        "AOK7_%=:\n\t"
        "ds_read_b128 v[96:99], v12 offset:20480\n\t"
        "ds_read_b64 v[120:121], v13 offset:20480\n\t"
        "ds_read_b128 v[112:115], v14 offset:3840\n\t"
        "ds_read_b128 v[116:119], v14 offset:4480\n\t"
        "s_setprio 3\n\t"
        "v_mfma_f32_32x32x16_bf16 v[52:67], v[108:111], v[0:3], 0\n\t"
        "v_exp_f32 v92, v68\n\t"
        "v_exp_f32 v93, v69\n\t"
        "v_exp_f32 v94, v70\n\t"
        "v_exp_f32 v95, v71\n\t"
        "v_cvt_pk_bf16_f32 v84, v92, v93\n\t"
        "v_cvt_pk_bf16_f32 v85, v94, v95\n\t"
        "v_mfma_f32_32x32x8_bf16 v[52:67], v[122:123], v[8:9], v[52:67]\n\t"
        "v_exp_f32 v92, v72\n\t"
        "v_exp_f32 v93, v73\n\t"
        "v_exp_f32 v94, v74\n\t"
        "v_exp_f32 v95, v75\n\t"
        "v_cvt_pk_bf16_f32 v86, v92, v93\n\t"
        "v_cvt_pk_bf16_f32 v87, v94, v95\n\t"
        "v_mfma_f32_32x32x16_bf16 v[20:35], v[104:107], v[88:91], v[20:35]\n\t"
        "v_exp_f32 v92, v76\n\t"
        "v_exp_f32 v93, v77\n\t"
        "v_exp_f32 v94, v78\n\t"
        "v_exp_f32 v95, v79\n\t"
        "v_cvt_pk_bf16_f32 v88, v92, v93\n\t"
        "v_cvt_pk_bf16_f32 v89, v94, v95\n\t"
        "v_mfma_f32_32x32x16_bf16 v[36:51], v[100:103], v[84:87], v[36:51]\n\t"
        "v_exp_f32 v92, v80\n\t"
        "v_exp_f32 v93, v81\n\t"
        "v_exp_f32 v94, v82\n\t"
        "v_exp_f32 v95, v83\n\t"
        "v_cvt_pk_bf16_f32 v90, v92, v93\n\t"
        "v_cvt_pk_bf16_f32 v91, v94, v95\n\t"
        "s_waitcnt lgkmcnt(0)\n\t"
        "v_mfma_f32_32x32x16_bf16 v[68:83], v[108:111], v[4:7], 0\n\t"
        "v_exp_f32 v92, v52\n\t"
        "v_exp_f32 v93, v53\n\t"
        "v_exp_f32 v94, v54\n\t"
        "v_exp_f32 v95, v55\n\t"
        "v_cvt_pk_bf16_f32 v84, v92, v93\n\t"
        "v_cvt_pk_bf16_f32 v85, v94, v95\n\t"
        "v_mfma_f32_32x32x8_bf16 v[68:83], v[122:123], v[10:11], v[68:83]\n\t"
        "v_exp_f32 v92, v56\n\t"
        "v_exp_f32 v93, v57\n\t"
        "v_exp_f32 v94, v58\n\t"
        "v_exp_f32 v95, v59\n\t"
        "v_cvt_pk_bf16_f32 v86, v92, v93\n\t"
        "v_cvt_pk_bf16_f32 v87, v94, v95\n\t"
        "v_mfma_f32_32x32x16_bf16 v[36:51], v[104:107], v[88:91], v[36:51]\n\t"
        "v_exp_f32 v92, v60\n\t"
        "v_exp_f32 v93, v61\n\t"
        "v_exp_f32 v94, v62\n\t"
        "v_exp_f32 v95, v63\n\t"
        "v_cvt_pk_bf16_f32 v88, v92, v93\n\t"
        "v_cvt_pk_bf16_f32 v89, v94, v95\n\t"
        "v_mfma_f32_32x32x16_bf16 v[20:35], v[112:115], v[84:87], v[20:35]\n\t"
        "v_exp_f32 v92, v64\n\t"
        "v_exp_f32 v93, v65\n\t"
        "v_exp_f32 v94, v66\n\t"
        "v_exp_f32 v95, v67\n\t"
        "v_cvt_pk_bf16_f32 v90, v92, v93\n\t"
        "v_cvt_pk_bf16_f32 v91, v94, v95\n\t"
        "ds_read_b128 v[108:111], v12 offset:21504\n\t"
        "ds_read_b64 v[122:123], v13 offset:20736\n\t"
        "ds_read_b128 v[100:103], v14 offset:20480\n\t"
        "ds_read_b128 v[104:107], v14 offset:21120\n\t"
        "s_setprio 2\n\t"
        "v_mfma_f32_32x32x16_bf16 v[52:67], v[96:99], v[0:3], 0\n\t"
        "v_exp_f32 v92, v68\n\t"
        "v_exp_f32 v93, v69\n\t"
        "v_exp_f32 v94, v70\n\t"
        "v_exp_f32 v95, v71\n\t"
        "v_cvt_pk_bf16_f32 v84, v92, v93\n\t"
        "v_cvt_pk_bf16_f32 v85, v94, v95\n\t"
        "v_mfma_f32_32x32x8_bf16 v[52:67], v[120:121], v[8:9], v[52:67]\n\t"
        "v_exp_f32 v92, v72\n\t"
        "v_exp_f32 v93, v73\n\t"
        "v_exp_f32 v94, v74\n\t"
        "v_exp_f32 v95, v75\n\t"
        "v_cvt_pk_bf16_f32 v86, v92, v93\n\t"
        "v_cvt_pk_bf16_f32 v87, v94, v95\n\t"
        "v_mfma_f32_32x32x16_bf16 v[20:35], v[116:119], v[88:91], v[20:35]\n\t"
        "v_exp_f32 v92, v76\n\t"
        "v_exp_f32 v93, v77\n\t"
        "v_exp_f32 v94, v78\n\t"
        "v_exp_f32 v95, v79\n\t"
        "v_cvt_pk_bf16_f32 v88, v92, v93\n\t"
        "v_cvt_pk_bf16_f32 v89, v94, v95\n\t"
        "v_mfma_f32_32x32x16_bf16 v[36:51], v[112:115], v[84:87], v[36:51]\n\t"
        "v_exp_f32 v92, v80\n\t"
        "v_exp_f32 v93, v81\n\t"
        "v_exp_f32 v94, v82\n\t"
        "v_exp_f32 v95, v83\n\t"
        "v_cvt_pk_bf16_f32 v90, v92, v93\n\t"
        "v_cvt_pk_bf16_f32 v91, v94, v95\n\t"
        "s_waitcnt lgkmcnt(0)\n\t"
        "v_mfma_f32_32x32x16_bf16 v[68:83], v[96:99], v[4:7], 0\n\t"
        "v_exp_f32 v92, v52\n\t"
        "v_exp_f32 v93, v53\n\t"
        "v_exp_f32 v94, v54\n\t"
        "v_exp_f32 v95, v55\n\t"
        "v_cvt_pk_bf16_f32 v84, v92, v93\n\t"
        "v_cvt_pk_bf16_f32 v85, v94, v95\n\t"
        "v_mfma_f32_32x32x8_bf16 v[68:83], v[120:121], v[10:11], v[68:83]\n\t"
        "v_exp_f32 v92, v56\n\t"
        "v_exp_f32 v93, v57\n\t"
        "v_exp_f32 v94, v58\n\t"
        "v_exp_f32 v95, v59\n\t"
        "v_cvt_pk_bf16_f32 v86, v92, v93\n\t"
        "v_cvt_pk_bf16_f32 v87, v94, v95\n\t"
        "v_mfma_f32_32x32x16_bf16 v[36:51], v[116:119], v[88:91], v[36:51]\n\t"
        "v_exp_f32 v92, v60\n\t"
        "v_exp_f32 v93, v61\n\t"
        "v_exp_f32 v94, v62\n\t"
        "v_exp_f32 v95, v63\n\t"
        "v_cvt_pk_bf16_f32 v88, v92, v93\n\t"
        "v_cvt_pk_bf16_f32 v89, v94, v95\n\t"
        "v_mfma_f32_32x32x16_bf16 v[20:35], v[100:103], v[84:87], v[20:35]\n\t"
        "v_exp_f32 v92, v64\n\t"
        "v_exp_f32 v93, v65\n\t"
        "v_exp_f32 v94, v66\n\t"
        "v_exp_f32 v95, v67\n\t"
        "v_cvt_pk_bf16_f32 v90, v92, v93\n\t"
        "v_cvt_pk_bf16_f32 v91, v94, v95\n\t"
        "ds_read_b128 v[96:99], v12 offset:22528\n\t"
        "ds_read_b64 v[120:121], v13 offset:20992\n\t"
        "ds_read_b128 v[112:115], v14 offset:21760\n\t"
        "ds_read_b128 v[116:119], v14 offset:22400\n\t"
        "s_setprio 1\n\t"
        "v_mfma_f32_32x32x16_bf16 v[52:67], v[108:111], v[0:3], 0\n\t"
        "v_exp_f32 v92, v68\n\t"
        "v_exp_f32 v93, v69\n\t"
        "v_exp_f32 v94, v70\n\t"
        "v_exp_f32 v95, v71\n\t"
        "v_cvt_pk_bf16_f32 v84, v92, v93\n\t"
        "v_cvt_pk_bf16_f32 v85, v94, v95\n\t"
        "v_mfma_f32_32x32x8_bf16 v[52:67], v[122:123], v[8:9], v[52:67]\n\t"
        "v_exp_f32 v92, v72\n\t"
        "v_exp_f32 v93, v73\n\t"
        "v_exp_f32 v94, v74\n\t"
        "v_exp_f32 v95, v75\n\t"
        "v_cvt_pk_bf16_f32 v86, v92, v93\n\t"
        "v_cvt_pk_bf16_f32 v87, v94, v95\n\t"
        "v_mfma_f32_32x32x16_bf16 v[20:35], v[104:107], v[88:91], v[20:35]\n\t"
        "v_exp_f32 v92, v76\n\t"
        "v_exp_f32 v93, v77\n\t"
        "v_exp_f32 v94, v78\n\t"
        "v_exp_f32 v95, v79\n\t"
        "v_cvt_pk_bf16_f32 v88, v92, v93\n\t"
        "v_cvt_pk_bf16_f32 v89, v94, v95\n\t"
        "v_mfma_f32_32x32x16_bf16 v[36:51], v[100:103], v[84:87], v[36:51]\n\t"
        "v_exp_f32 v92, v80\n\t"
        "v_exp_f32 v93, v81\n\t"
        "v_exp_f32 v94, v82\n\t"
        "v_exp_f32 v95, v83\n\t"
        "v_cvt_pk_bf16_f32 v90, v92, v93\n\t"
        "v_cvt_pk_bf16_f32 v91, v94, v95\n\t"
        "s_waitcnt lgkmcnt(0)\n\t"
        "v_mfma_f32_32x32x16_bf16 v[68:83], v[108:111], v[4:7], 0\n\t"
        "v_exp_f32 v92, v52\n\t"
        "v_exp_f32 v93, v53\n\t"
        "v_exp_f32 v94, v54\n\t"
        "v_exp_f32 v95, v55\n\t"
        "v_cvt_pk_bf16_f32 v84, v92, v93\n\t"
        "v_cvt_pk_bf16_f32 v85, v94, v95\n\t"
        "v_mfma_f32_32x32x8_bf16 v[68:83], v[122:123], v[10:11], v[68:83]\n\t"
        "v_exp_f32 v92, v56\n\t"
        "v_exp_f32 v93, v57\n\t"
        "v_exp_f32 v94, v58\n\t"
        "v_exp_f32 v95, v59\n\t"
        "v_cvt_pk_bf16_f32 v86, v92, v93\n\t"
        "v_cvt_pk_bf16_f32 v87, v94, v95\n\t"
        "v_mfma_f32_32x32x16_bf16 v[36:51], v[104:107], v[88:91], v[36:51]\n\t"
        "v_exp_f32 v92, v60\n\t"
        "v_exp_f32 v93, v61\n\t"
        "v_exp_f32 v94, v62\n\t"
        "v_exp_f32 v95, v63\n\t"
        "v_cvt_pk_bf16_f32 v88, v92, v93\n\t"
        "v_cvt_pk_bf16_f32 v89, v94, v95\n\t"
        "v_mfma_f32_32x32x16_bf16 v[20:35], v[112:115], v[84:87], v[20:35]\n\t"
        "v_exp_f32 v92, v64\n\t"
        "v_exp_f32 v93, v65\n\t"
        "v_exp_f32 v94, v66\n\t"
        "v_exp_f32 v95, v67\n\t"
        "v_cvt_pk_bf16_f32 v90, v92, v93\n\t"
        "v_cvt_pk_bf16_f32 v91, v94, v95\n\t"
        "ds_read_b128 v[108:111], v12 offset:23552\n\t"
        "ds_read_b64 v[122:123], v13 offset:21248\n\t"
        "ds_read_b128 v[100:103], v14 offset:23040\n\t"
        "ds_read_b128 v[104:107], v14 offset:23680\n\t"
        "s_setprio 0\n\t"
        "v_mfma_f32_32x32x16_bf16 v[52:67], v[96:99], v[0:3], 0\n\t"
        "v_exp_f32 v92, v68\n\t"
        "v_exp_f32 v93, v69\n\t"
        "v_exp_f32 v94, v70\n\t"
        "v_exp_f32 v95, v71\n\t"
        "v_cvt_pk_bf16_f32 v84, v92, v93\n\t"
        "v_cvt_pk_bf16_f32 v85, v94, v95\n\t"
        "v_mfma_f32_32x32x8_bf16 v[52:67], v[120:121], v[8:9], v[52:67]\n\t"
        "v_exp_f32 v92, v72\n\t"
        "v_exp_f32 v93, v73\n\t"
        "v_exp_f32 v94, v74\n\t"
        "v_exp_f32 v95, v75\n\t"
        "v_cvt_pk_bf16_f32 v86, v92, v93\n\t"
        "v_cvt_pk_bf16_f32 v87, v94, v95\n\t"
        "v_mfma_f32_32x32x16_bf16 v[20:35], v[116:119], v[88:91], v[20:35]\n\t"
        "v_exp_f32 v92, v76\n\t"
        "v_exp_f32 v93, v77\n\t"
        "v_exp_f32 v94, v78\n\t"
        "v_exp_f32 v95, v79\n\t"
        "v_cvt_pk_bf16_f32 v88, v92, v93\n\t"
        "v_cvt_pk_bf16_f32 v89, v94, v95\n\t"
        "v_mfma_f32_32x32x16_bf16 v[36:51], v[112:115], v[84:87], v[36:51]\n\t"
        "v_exp_f32 v92, v80\n\t"
        "v_exp_f32 v93, v81\n\t"
        "v_exp_f32 v94, v82\n\t"
        "v_exp_f32 v95, v83\n\t"
        "v_cvt_pk_bf16_f32 v90, v92, v93\n\t"
        "v_cvt_pk_bf16_f32 v91, v94, v95\n\t"
        "s_waitcnt lgkmcnt(0)\n\t"
        "v_mfma_f32_32x32x16_bf16 v[68:83], v[96:99], v[4:7], 0\n\t"
        "v_exp_f32 v92, v52\n\t"
        "v_exp_f32 v93, v53\n\t"
        "v_exp_f32 v94, v54\n\t"
        "v_exp_f32 v95, v55\n\t"
        "v_cvt_pk_bf16_f32 v84, v92, v93\n\t"
        "v_cvt_pk_bf16_f32 v85, v94, v95\n\t"
        "v_mfma_f32_32x32x8_bf16 v[68:83], v[120:121], v[10:11], v[68:83]\n\t"
        "v_exp_f32 v92, v56\n\t"
        "v_exp_f32 v93, v57\n\t"
        "v_exp_f32 v94, v58\n\t"
        "v_exp_f32 v95, v59\n\t"
        "v_cvt_pk_bf16_f32 v86, v92, v93\n\t"
        "v_cvt_pk_bf16_f32 v87, v94, v95\n\t"
        "v_mfma_f32_32x32x16_bf16 v[36:51], v[116:119], v[88:91], v[36:51]\n\t"
        "v_exp_f32 v92, v60\n\t"
        "v_exp_f32 v93, v61\n\t"
        "v_exp_f32 v94, v62\n\t"
        "v_exp_f32 v95, v63\n\t"
        "v_cvt_pk_bf16_f32 v88, v92, v93\n\t"
        "v_cvt_pk_bf16_f32 v89, v94, v95\n\t"
        "v_mfma_f32_32x32x16_bf16 v[20:35], v[100:103], v[84:87], v[20:35]\n\t"
        "v_exp_f32 v92, v64\n\t"
        "v_exp_f32 v93, v65\n\t"
        "v_exp_f32 v94, v66\n\t"
        "v_exp_f32 v95, v67\n\t"
        "v_cvt_pk_bf16_f32 v90, v92, v93\n\t"
        "v_cvt_pk_bf16_f32 v91, v94, v95\n\t"
        "ds_read_b128 v[112:115], v14 offset:24320\n\t"
        "ds_read_b128 v[116:119], v14 offset:24960\n\t"
        "s_setprio 3\n\t"
        "v_mfma_f32_32x32x16_bf16 v[52:67], v[108:111], v[0:3], 0\n\t"
        "v_exp_f32 v92, v68\n\t"
        "v_exp_f32 v93, v69\n\t"
        "v_exp_f32 v94, v70\n\t"
        "v_exp_f32 v95, v71\n\t"
        "v_cvt_pk_bf16_f32 v84, v92, v93\n\t"
        "v_cvt_pk_bf16_f32 v85, v94, v95\n\t"
        "v_mfma_f32_32x32x8_bf16 v[52:67], v[122:123], v[8:9], v[52:67]\n\t"
        "v_exp_f32 v92, v72\n\t"
        "v_exp_f32 v93, v73\n\t"
        "v_exp_f32 v94, v74\n\t"
        "v_exp_f32 v95, v75\n\t"
        "v_cvt_pk_bf16_f32 v86, v92, v93\n\t"
        "v_cvt_pk_bf16_f32 v87, v94, v95\n\t"
        "v_mfma_f32_32x32x16_bf16 v[20:35], v[104:107], v[88:91], v[20:35]\n\t"
        "v_exp_f32 v92, v76\n\t"
        "v_exp_f32 v93, v77\n\t"
        "v_exp_f32 v94, v78\n\t"
        "v_exp_f32 v95, v79\n\t"
        "v_cvt_pk_bf16_f32 v88, v92, v93\n\t"
        "v_cvt_pk_bf16_f32 v89, v94, v95\n\t"
        "v_mfma_f32_32x32x16_bf16 v[36:51], v[100:103], v[84:87], v[36:51]\n\t"
        "v_exp_f32 v92, v80\n\t"
        "v_exp_f32 v93, v81\n\t"
        "v_exp_f32 v94, v82\n\t"
        "v_exp_f32 v95, v83\n\t"
        "v_cvt_pk_bf16_f32 v90, v92, v93\n\t"
        "v_cvt_pk_bf16_f32 v91, v94, v95\n\t"
        "s_waitcnt lgkmcnt(0)\n\t"
        "v_mfma_f32_32x32x16_bf16 v[68:83], v[108:111], v[4:7], 0\n\t"
        "v_exp_f32 v92, v52\n\t"
        "v_exp_f32 v93, v53\n\t"
        "v_exp_f32 v94, v54\n\t"
        "v_exp_f32 v95, v55\n\t"
        "v_cvt_pk_bf16_f32 v84, v92, v93\n\t"
        "v_cvt_pk_bf16_f32 v85, v94, v95\n\t"
        "v_mfma_f32_32x32x8_bf16 v[68:83], v[122:123], v[10:11], v[68:83]\n\t"
        "v_exp_f32 v92, v56\n\t"
        "v_exp_f32 v93, v57\n\t"
        "v_exp_f32 v94, v58\n\t"
        "v_exp_f32 v95, v59\n\t"
        "v_cvt_pk_bf16_f32 v86, v92, v93\n\t"
        "v_cvt_pk_bf16_f32 v87, v94, v95\n\t"
        "v_mfma_f32_32x32x16_bf16 v[36:51], v[104:107], v[88:91], v[36:51]\n\t"
        "v_exp_f32 v92, v60\n\t"
        "v_exp_f32 v93, v61\n\t"
        "v_exp_f32 v94, v62\n\t"
        "v_exp_f32 v95, v63\n\t"
        "v_cvt_pk_bf16_f32 v88, v92, v93\n\t"
        "v_cvt_pk_bf16_f32 v89, v94, v95\n\t"
        "v_mfma_f32_32x32x16_bf16 v[20:35], v[112:115], v[84:87], v[20:35]\n\t"
        "v_exp_f32 v92, v64\n\t"
        "v_exp_f32 v93, v65\n\t"
        "v_exp_f32 v94, v66\n\t"
        "v_exp_f32 v95, v67\n\t"
        "v_cvt_pk_bf16_f32 v90, v92, v93\n\t"
        "v_cvt_pk_bf16_f32 v91, v94, v95\n\t"
        "s_setprio 2\n\t"
        "v_exp_f32 v92, v68\n\t"
        "v_exp_f32 v93, v69\n\t"
        "v_exp_f32 v94, v70\n\t"
        "v_exp_f32 v95, v71\n\t"
        "v_cvt_pk_bf16_f32 v84, v92, v93\n\t"
        "v_cvt_pk_bf16_f32 v85, v94, v95\n\t"
        "v_exp_f32 v92, v72\n\t"
        "v_exp_f32 v93, v73\n\t"
        "v_exp_f32 v94, v74\n\t"
        "v_exp_f32 v95, v75\n\t"
        "v_cvt_pk_bf16_f32 v86, v92, v93\n\t"
        "v_cvt_pk_bf16_f32 v87, v94, v95\n\t"
        "v_mfma_f32_32x32x16_bf16 v[20:35], v[116:119], v[88:91], v[20:35]\n\t"
        "v_exp_f32 v92, v76\n\t"
        "v_exp_f32 v93, v77\n\t"
        "v_exp_f32 v94, v78\n\t"
        "v_exp_f32 v95, v79\n\t"
        "v_cvt_pk_bf16_f32 v88, v92, v93\n\t"
        "v_cvt_pk_bf16_f32 v89, v94, v95\n\t"
        "v_mfma_f32_32x32x16_bf16 v[36:51], v[112:115], v[84:87], v[36:51]\n\t"
        "v_exp_f32 v92, v80\n\t"
        "v_exp_f32 v93, v81\n\t"
        "v_exp_f32 v94, v82\n\t"
        "v_exp_f32 v95, v83\n\t"
        "v_cvt_pk_bf16_f32 v90, v92, v93\n\t"
        "v_cvt_pk_bf16_f32 v91, v94, v95\n\t"
        "s_nop 1\n\t"
        "v_mfma_f32_32x32x16_bf16 v[36:51], v[116:119], v[88:91], v[36:51]\n\t"
        "s_nop 7\n\t"
        "s_nop 7\n\t"
        "s_setprio 0\n\t"
        "s_mov_b32 m0, %[sv]\n\t"
        : "={v[20:35]}"(o[0]), "={v[36:51]}"(o[1]), [sv] "=&s"(sv), [t] "=&s"(t), "+{s[40:41]}"(gb)
        : "{v[0:3]}"(q0[0]), "{v[8:9]}"(q1[0]), "{v[4:7]}"(q0[1]), "{v[10:11]}"(q1[1]), "{v12}"(aK0), "{v13}"(aK1), "{v14}"(aV), "{v15}"(g0), "{v16}"(g1), "{v17}"(g2), [m0] "s"(m0), [m1] "s"(m1), [m2] "s"(m2), [cn] "s"(cn)
        : "v52", "v53", "v54", "v55", "v56", "v57", "v58", "v59", "v60", "v61", "v62", "v63", "v64", "v65", "v66", "v67", "v68", "v69", "v70", "v71", "v72", "v73", "v74", "v75", "v76", "v77", "v78", "v79", "v80", "v81", "v82", "v83", "v84", "v85", "v86", "v87", "v88", "v89", "v90", "v91", "v92", "v93", "v94", "v95", "v18", "v96", "v97", "v98", "v99", "v120", "v121", "v100", "v101", "v102", "v103", "v104", "v105", "v106", "v107", "v108", "v109", "v110", "v111", "v122", "v123", "v112", "v113", "v114", "v115", "v116", "v117", "v118", "v119", "memory", "scc");
}

__device__ __forceinline__ void p2_attn(int b, int w, char* __restrict__ ws, rsrc_t R, unsigned lds0, unsigned epoch STARG) {
    const int lane = lane_id(), r = lane & 31, hi = lane >> 5;
    const int xcd = b & 7, i = b >> 3, h = xcd * 4 + (i >> 3), qb = i & 7;
    const int qg = w & 3, kh = w >> 2;
    const unsigned ring = lds0 + L_ATT;
    const char* kvh = ws + WS_KV + (size_t)h * 16 * KVG;
    if (w == 0) {
        const int sec = lane >> 4, c0 = sec * 576 + 18 * h;
        const int ta = c0 >> 7, tb = (c0 + 17) >> 7;
        poll(ws, FL_P1 + (ta * 16 + (lane & 15)) * 4, lane < 48, FL_P1 + (tb * 16 + (lane & 15)) * 4, lane < 48, epoch);
        if (lane == 0) ldswr<unsigned>(lds0 + L_SYNC + SY_GO * 4, 1u);
    } else lds_wait(lds0 + L_SYNC + SY_GO * 4, 1);
    ST(10);
    unsigned voff[3], m0b[3];
#pragma unroll
    for (int k = 0; k < 3; ++k) {
        int p = w + 8 * k; if (p >= 20) p -= 20;
        const int half = p >= 10 ? 1 : 0, o = (p - 10 * half) * 1024;
        voff[k] = half * 8 * KVG + o + lane * 16;
        m0b[k] = ring + half * KVG + o;
    }
    lds_wait(lds0 + L_SYNC + SY_C1X * 4, b < 224 ? 4 : 8);
#pragma unroll
    for (int k = 0; k < 3; ++k) glds16_sc1(kvh + voff[k], rfl(m0b[k]));
    const int tileA = qb * 8 + 2 * qg;
    u32x4 q0[2]; u32x2 q1[2];
#pragma unroll
    for (int j = 0; j < 2; ++j) { q0[j] = ld128(R, WS_QF0 + (h * 64 + tileA + j) * 1024 + lane * 16); q1[j] = ld64(R, WS_QF1 + (h * SEQ + (tileA + j) * 32 + r) * 8); }
    float k2 = 0.f;
    {
        float km[3];
#pragma unroll
        for (int g = 0; g < 3; ++g) km[g] = __uint_as_float(ld32(R, WS_KMX + ((h * 5 + (g < 2 ? 2 * g + hi : 4)) * 32 + r) * 4));
#pragma unroll
        for (int g = 0; g < 3; ++g) { const unsigned m = __float_as_uint(half_max_nn(km[g])); k2 += __uint_as_float(__builtin_amdgcn_readlane(m, 31)); if (g < 2) k2 += __uint_as_float(__builtin_amdgcn_readlane(m, 63)); }
    }
#pragma unroll
    for (int k = 0; k < 3; ++k) glds16_sc1(kvh + KVG + voff[k], rfl(m0b[k] + 2 * KVG));
    float bnd[2]; bool unsafe = false;
#pragma unroll
    for (int j = 0; j < 2; ++j) {
        float n2 = 0.f;
#pragma unroll
        for (int e = 0; e < 4; ++e) { const float a = bflo(q0[j][e]), c = bfhi(q0[j][e]); n2 += a * a + c * c; }
        n2 += swap32(n2);
        const float a = bflo(q1[j][0]), c = bfhi(q1[j][0]); n2 += a * a + c * c;
        bnd[j] = __builtin_amdgcn_sqrtf(n2 * k2) * 1.01f + 1e-30f;
        unsafe = unsafe || !(bnd[j] <= SAFE_B);
    }
    const int wbad = __any(unsafe) ? 1 : 0;
    if (lane == 0) ldswr<int>(lds0 + L_SYNC + (SY_BAD + w) * 4, wbad);
#pragma unroll
    for (int j = 0; j < 2; ++j) { q1[j][1] = cvtpk(-bnd[j], 0.f) & 0xffffu; if (hi) q1[j] = u32x2{0, 0}; }
    asm volatile("s_waitcnt vmcnt(3)" ::: "memory");
    sw_barrier(lds0 + L_SYNC + SY_ATT * 4, 8);
    ST(11);
    const u32x4 f0 = ldsrd<u32x4>(lds0 + L_SYNC + SY_BAD * 4), f1 = ldsrd<u32x4>(lds0 + L_SYNC + SY_BAD * 4 + 16);
    const int bad = rfl(f0[0] | f0[1] | f0[2] | f0[3] | f1[0] | f1[1] | f1[2] | f1[3]);
    const int vrow = (r < 19) ? r : 19;
    const unsigned aK0 = ring + kh * KVG + lane * 16, aK1 = ring + kh * KVG + 4096 + r * 8, aV = ring + kh * KVG + 5120 + hi * 320 + vrow * 16;
    f32x16 o[2]; float mx[2] = {0.f, 0.f};
    if (!bad) {
        const uintptr_t gb = (uintptr_t)(kvh + 2 * KVG);
        attn_fast(o, q0, q1, aK0, aK1, aV, voff[0], voff[1], voff[2], rfl(m0b[0]), rfl(m0b[1]), rfl(m0b[2]),
                  ((unsigned long long)rfl((unsigned)(gb >> 32)) << 32) | rfl((unsigned)gb), rfl(lds0 + L_SYNC + SY_ATT * 4));
    } else {
        o[0] = f32x16{}; o[1] = f32x16{}; mx[0] = -INFINITY; mx[1] = -INFINITY;
        const bf16x8 Q0[2] = {__builtin_bit_cast(bf16x8, q0[0]), __builtin_bit_cast(bf16x8, q0[1])};
        const bf16x4 Q1[2] = {__builtin_bit_cast(bf16x4, (u32x2{q1[0][0], 0u})), __builtin_bit_cast(bf16x4, (u32x2{q1[1][0], 0u}))};
#pragma unroll 1
        for (int s = 0; s < 8; ++s) {
            if (s >= 1) {
                WAITV0();
                sw_barrier(lds0 + L_SYNC + (SY_ATT + s) * 4, 8);
                if (s + 1 < 8) {
#pragma unroll
                    for (int k = 0; k < 3; ++k) glds16_sc1(kvh + (s + 1) * KVG + voff[k], rfl(m0b[k] + ((s + 1) % 3) * 2 * KVG));
                }
            }
            const unsigned sb = (s % 3) * 2 * KVG;
#pragma unroll 1
            for (int t = 0; t < 4; ++t) {
                const bf16x8 k0 = ldsrd<bf16x8>(aK0 + sb + t * 1024); const bf16x4 k1 = ldsrd<bf16x4>(aK1 + sb + t * 256);
                const bf16x8 v0 = ldsrd<bf16x8>(aV + sb + 2 * t * 640), v1 = ldsrd<bf16x8>(aV + sb + (2 * t + 1) * 640);
#pragma unroll
                for (int j = 0; j < 2; ++j) {
                    f32x16 sc = MFMA32(k0, Q0[j], f32x16{}); sc = MFMA32K8(k1, Q1[j], sc);
                    float m = sc[0];
#pragma unroll
                    for (int e = 1; e < 16; ++e) m = fmaxf(m, sc[e]);
                    m = fmaxf(m, swap32(m));
                    const float mn = fmaxf(mx[j], m);
                    const float msafe = (mn == -INFINITY) ? 0.f : mn;
                    const float al = __builtin_amdgcn_exp2f(mx[j] - msafe);
                    mx[j] = mn;
                    u32x4 pa, pb;
#pragma unroll
                    for (int e = 0; e < 4; ++e) {
                        pa[e] = cvtpk(__builtin_amdgcn_exp2f(sc[2 * e] - msafe), __builtin_amdgcn_exp2f(sc[2 * e + 1] - msafe));
                        pb[e] = cvtpk(__builtin_amdgcn_exp2f(sc[8 + 2 * e] - msafe), __builtin_amdgcn_exp2f(sc[8 + 2 * e + 1] - msafe));
                    }
#pragma unroll
                    for (int e = 0; e < 16; ++e) o[j][e] *= al;
                    o[j] = MFMA32(v0, __builtin_bit_cast(bf16x8, pa), o[j]); o[j] = MFMA32(v1, __builtin_bit_cast(bf16x8, pb), o[j]);
                }
            }
        }
    }
    ST(12);
    sw_barrier(lds0 + L_SYNC + (SY_MB + 0) * 4, 8);
    int ln = lane_id(); asm volatile("" : "+v"(ln));
    const unsigned mg = ring + (qg * 2) * (13 * 256) + ln * 4;
    {
        const f32x16& og = kh ? o[0] : o[1]; const float mgv = kh ? mx[0] : mx[1];
        const unsigned dst = mg + (kh ? 0 : 13 * 256);
#pragma unroll
        for (int e = 0; e < 12; ++e) ldswr<float>(dst + e * 256, og[e]);
        ldswr<float>(dst + 12 * 256, mgv);
    }
    sw_barrier(lds0 + L_SYNC + (SY_MB + 1) * 4, 8);
    {
        const f32x16& oo = kh ? o[1] : o[0]; const float m1 = kh ? mx[1] : mx[0];
        const unsigned src = mg + (kh ? 13 * 256 : 0);
        float f1 = 1.f, f2 = 1.f;
        if (bad) { const float m2 = ldsrd<float>(src + 12 * 256); const float mn = fmaxf(m1, m2); f1 = __builtin_amdgcn_exp2f(m1 - mn); f2 = __builtin_amdgcn_exp2f(m2 - mn); }
        float v[12];
#pragma unroll
        for (int e = 0; e < 12; ++e) v[e] = oo[e] * f1 + ldsrd<float>(src + e * 256) * f2;
        const auto rr = __builtin_amdgcn_permlane32_swap(__float_as_uint(v[10]), __float_as_uint(v[10]), false, false);
        const float inv = 1.0f / __uint_as_float(rr[0]);
        const int tok = (qb * 8 + 2 * qg + kh) * 32 + (ln & 31);
        st128(R, WS_OM + (h * SEQ + tok) * 32 + (ln >> 5) * 16, u32x4{cvtpk(v[0] * inv, v[1] * inv), cvtpk(v[2] * inv, v[3] * inv), cvtpk(v[4] * inv, v[5] * inv), cvtpk(v[6] * inv, v[7] * inv)});
        if (ln < 32) st32(R, WS_OR + (h * SEQ + tok) * 4, cvtpk(v[8] * inv, v[9] * inv));
    }
    publish(ws, FL_O + (qb * 32 + h) * 4, lds0 + L_SYNC + SY_O * 4, 8, epoch);
    ST(13);
}

__device__ __forceinline__ void p3_out(int b, int w, char* __restrict__ ws, rsrc_t R, u16* __restrict__ out, unsigned lds0, unsigned epoch STARG) {
    int lane = lane_id(); asm volatile("" : "+v"(lane));
    const int tid = w * 64 + lane;
    const int xcd = b & 7, idx = b >> 3;
    const int tb = (xcd >> 1) * 8 + (idx & 7), cg = (xcd & 1) * 4 + (idx >> 3);
    const int t0 = tb * 64, n0 = cg * 72;
    const unsigned stg = lds0 + L_ATT + 32768;
    if (w == 0) {
        poll(ws, FL_O + ((tb >> 2) * 32 + (lane & 31)) * 4, lane < 32, 0, false, epoch);
        if (lane == 0) ldswr<unsigned>(lds0 + L_SYNC + SY_GO2 * 4, 1u);
    } else lds_wait(lds0 + L_SYNC + SY_GO2 * 4, 1);
    ST(15);
    const int tt = w & 3, kh = w >> 2, c16 = lane & 15, g = lane >> 4;
    const int row = tt * 16 + c16, tok = t0 + row;
    bf16x8 of[9];
#pragma unroll
    for (int ks = 0; ks < 9; ++ks) {
        const int kg = kh * 9 + ks;
        if (kh == 0 || ks < 7) of[ks] = __builtin_bit_cast(bf16x8, ld128(R, WS_OM + ((2 * kg + (g >> 1)) * SEQ + tok) * 32 + (g & 1) * 16));
        else { const unsigned o4 = WS_OR + ((16 * (kg - 16) + 4 * g) * SEQ + tok) * 4;
               of[ks] = __builtin_bit_cast(bf16x8, (u32x4{ld32(R, o4), ld32(R, o4 + SEQ * 4), ld32(R, o4 + 2 * SEQ * 4), ld32(R, o4 + 3 * SEQ * 4)})); }
    }
    lds_wait(lds0 + L_SYNC + SY_WO * 4, 8);
    f32x4 acc[5];
#pragma unroll
    for (int j = 0; j < 5; ++j) acc[j] = f32x4{};
    const int q = c16 >> 2, p = lane & 3;
    const unsigned wa0 = lds0 + (kh * 288 + 8 * g + q) * 144 + 4 * p * 2;
#pragma unroll
    for (int ks = 0; ks < 9; ++ks) {
#pragma unroll
        for (int j = 0; j < 5; ++j) {
            const int nb = (j < 4) ? j * 16 : 56;
            const unsigned a = wa0 + ks * 32 * 144 + nb * 2;
            const bf16x4 lo = trread(a), hh = trread(a + 4 * 144);
            acc[j] = MFMA16((bf16x8{lo[0], lo[1], lo[2], lo[3], hh[0], hh[1], hh[2], hh[3]}), of[ks], acc[j]);
        }
    }
    ST(16);
    const unsigned red = lds0 + L_ATT + tt * (5 * 1024) + lane * 16;
    if (kh == 1) {
#pragma unroll
        for (int j = 0; j < 5; ++j) ldswr<f32x4>(red + j * 1024, acc[j]);
    }
    sw_barrier(lds0 + L_SYNC + (SY_MB + 2) * 4, 8);
    if (kh == 0) {
#pragma unroll
        for (int j = 0; j < 5; ++j) {
            const f32x4 o = ldsrd<f32x4>(red + j * 1024);
            const int nb = (j < 4) ? j * 16 : 56;
            ldswr<u32x2>(stg + row * 144 + (nb + 4 * g) * 2, u32x2{cvtpk(acc[j][0] + o[0], acc[j][1] + o[1]), cvtpk(acc[j][2] + o[2], acc[j][3] + o[3])});
        }
    }
    sw_barrier(lds0 + L_SYNC + (SY_MB + 3) * 4, 8);
    for (int it = tid; it < 576; it += 512) {
        const int tk = it / 9, cc = it % 9;
        *(u32x4*)(out + (size_t)(t0 + tk) * 1152 + n0 + cc * 8) = ldsrd<u32x4>(stg + tk * 144 + cc * 16);
    }
}

__global__ __launch_bounds__(1024) void fused_kernel(const u16* __restrict__ x, const u16* __restrict__ wqkv, const u16* __restrict__ wo, const u16* __restrict__ bq,
                                                     const u16* __restrict__ bk, const u16* __restrict__ bv, const u16* __restrict__ shiftk, const u16* __restrict__ ssmk,
                                                     const u16* __restrict__ ssmD, u16* __restrict__ out, char* __restrict__ ws) {
    __shared__ __attribute__((aligned(128))) char lds[LDS_BYTES];
    const unsigned lds0 = (unsigned)(uintptr_t)lds;
    const int tid = threadIdx.x, b = blockIdx.x;
    const int w = rfl(tid >> 6);
    ST(0); STC(20);
    const rsrc_t R = __builtin_amdgcn_make_buffer_rsrc(ws, 0, WS_END, 0x00020000);
    if (w == 8) { ldswr<u32x2>(lds0 + L_SYNC + lane_id() * 8, u32x2{0u, 0u}); asm volatile("s_waitcnt lgkmcnt(0)" ::: "memory"); }
    asm volatile("s_barrier" ::: "memory");
    if (w == 8 && lane_id() == 0) {
        ldswr<unsigned>(lds0 + L_SYNC + SY_EPOCH * 4, flag_ld(ws, WS_FLG) + 1u);
        asm volatile("s_waitcnt lgkmcnt(0)" ::: "memory");
        ldswr<unsigned>(lds0 + L_SYNC + SY_EPRDY * 4, 1u);
    }
    const bool M = w < 8;
    if (b < 224) {
        if (w < 4) p1_gemm(b, w, x, wqkv, ws, R, lds0 STPASS);
        else if ((w & 4) != 0) p1_loader(w, (w & 3) + (M ? 0 : 4), b, x, wqkv, lds0 STPASS);
        else c1_kv<4>(w, b, (w - 8) * 64 + lane_id(), bk, bv, shiftk, ws, R, lds0 + L_C1X, lds0 + L_SYNC + SY_C1X * 4, lds0 + L_SYNC + SY_BC1X * 4, lds0 STPASS);
    } else {
        c1_kv<8>(w, M ? 256 + b - 224 : b, (w & 7) * 64 + lane_id(), bk, bv, shiftk, ws, R, lds0 + (M ? L_C1M : L_C1X), lds0 + L_SYNC + (M ? SY_C1M : SY_C1X) * 4,
                 lds0 + L_SYNC + (M ? SY_BC1M : SY_BC1X) * 4, lds0 STPASS);
    }
    if (!M || b >= 224) {
        ST(5);
        const int cs = b < 224 ? (w - 8) >> 2 : w >> 2;
        c2_conv(w, b < 224 ? 2 * b + cs : 448 + 4 * (b - 224) + cs, cs, w & 3, b < 224 ? 8 : 16, b, ssmk, ssmD, ws, R, lds0, get_epoch(lds0) STPASS);
        if (!M) {
            x_tail(b, w - 8, b < 224 ? 8 : 16, wo, bq, ws, R, out, lds0, get_epoch(lds0) STPASS);
            STC(21); STDUMP();
            return;
        }
    }
    const unsigned epoch = get_epoch(lds0);
    p2_attn(b, w, ws, R, lds0, epoch STPASS);
    if (b == 0 && w == 7) {
        const int ln = lane_id();
        poll(ws, FL_C2 + ln * 4, true, FL_C2 + (64 + ln) * 4, true, epoch);
        poll(ws, FL_C2 + (128 + ln) * 4, true, FL_C2 + (192 + ln) * 4, true, epoch);
        if (ln == 0) flag_st(ws, WS_FLG, epoch);
    }
    p3_out(b, w, ws, R, out, lds0, epoch STPASS);
    ST(17); STC(21); STDUMP();
}

extern "C" void kernel_launch(void* const* d_in, const int* in_sizes, int n_in, void* d_out, int out_size, void* d_ws, size_t ws_size, hipStream_t stream) {
    fused_kernel<<<256, 1024, 0, stream>>>((const u16*)d_in[0], (const u16*)d_in[1], (const u16*)d_in[2], (const u16*)d_in[3], (const u16*)d_in[4], (const u16*)d_in[5],
                                           (const u16*)d_in[6], (const u16*)d_in[7], (const u16*)d_in[8], (u16*)d_out, (char*)d_ws);
}
```
